```python
import torch

def _softmax(x, axis=-1):
    return torch.softmax(x, dim=axis)
def _gelu(x):
    return torch.nn.functional.gelu(x, approximate="tanh")
def _silu(x):
    return torch.nn.functional.silu(x)
def _sigmoid(x):
    return torch.sigmoid(x)
def _layernorm(x, axis=-1, eps=1e-5):
    m = x.mean(dim=axis, keepdim=True)
    v = x.var(dim=axis, keepdim=True, unbiased=False)
    return (x - m) / torch.sqrt(v + eps)
def _scatter_add(tgt, idx, src, axis=0):
    return tgt.clone().index_add_(axis, idx, src)
def _segment_sum(src, seg, n):
    out = torch.zeros((n,) + src.shape[1:], dtype=src.dtype)
    return out.index_add_(0, seg, src)
def _pad(x, pw):
    flat = tuple(v for pair in reversed(pw) for v in pair)
    return torch.nn.functional.pad(x, flat)

kernel_name = 'gated_mix__grouped_kv_mqa__spectral_fnet__prompt'

def setup_inputs():

    _w = (lambda a: (lambda t: t.to(torch.bfloat16) if t.is_floating_point() else t)(torch.as_tensor(a)))
    def _A_setup():
        B, S, D, H = 1, 2048, 640, 32
        Dh = D // H
        return {
            "x": _w((lambda *s: torch.randn(*s, dtype=torch.bfloat16) * 0.05)(B, S, D)),
            "wq": _w((lambda *s: torch.randn(*s, dtype=torch.bfloat16) * 1.6)(D, H * Dh)),
            "wk": _w((lambda *s: torch.randn(*s, dtype=torch.bfloat16) * 1.6)(D, Dh)),
            "wv": _w((lambda *s: torch.randn(*s, dtype=torch.bfloat16) * 1.6)(D, Dh)),
            "wo": _w((lambda *s: torch.randn(*s, dtype=torch.bfloat16) * 1.6)(H * Dh, D)),
        }
    _A_in = _A_setup()

    def _B_setup():
        B, S, D = 1, 2048, 640
        ns = torch.arange(S)[:, None] * torch.arange(S)[None, :]
        nd = torch.arange(D)[:, None] * torch.arange(D)[None, :]
        Ws_re = torch.cos(-2.0 * torch.pi * ns / S).to(float)
        Ws_im = torch.sin(-2.0 * torch.pi * ns / S).to(float)
        Wd_re = torch.cos(-2.0 * torch.pi * nd / D).to(float)
        Wd_im = torch.sin(-2.0 * torch.pi * nd / D).to(float)
        return {
            "x": _w((lambda *s: torch.randn(*s, dtype=torch.bfloat16) * 0.05)(B, S, D)),
            "dft_seq_re": _w(Ws_re),
            "dft_seq_im": _w(Ws_im),
            "dft_hid_re": _w(Wd_re),
            "dft_hid_im": _w(Wd_im),
        }
    _B_in = _B_setup()

    return {
        "A_x": _A_in["x"],
        "A_wq": _A_in["wq"],
        "A_wk": _A_in["wk"],
        "A_wv": _A_in["wv"],
        "A_wo": _A_in["wo"],
        "B_x": _B_in["x"],
        "B_dft_seq_re": _B_in["dft_seq_re"],
        "B_dft_seq_im": _B_in["dft_seq_im"],
        "B_dft_hid_re": _B_in["dft_hid_re"],
        "B_dft_hid_im": _B_in["dft_hid_im"],
        "gate_w": _w((lambda *s: torch.randn(*s, dtype=torch.bfloat16) * 0.05)(640) * 16.0),
    }

def reference(A_x, A_wq, A_wk, A_wv, A_wo, B_x, B_dft_seq_re, B_dft_seq_im, B_dft_hid_re, B_dft_hid_im, gate_w):
    def _A_ref(x, wq, wk, wv, wo):
        B, S, D = x.shape
        H = 32
        Dh = D // H
        q = (lambda x, *d: x.permute(*d))((x @ wq).reshape(B, S, H, Dh), 0, 2, 1, 3)
        k = x @ wk
        v = x @ wv
        scores = torch.einsum("bhqd,bkd->bhqk", q, k) * (Dh ** -0.5)
        p = _softmax(scores, axis=-1)
        o = torch.einsum("bhqk,bkd->bhqd", p, v)
        return (lambda x, *d: x.permute(*d))(o, 0, 2, 1, 3).reshape(B, S, H * Dh) @ wo

    def _B_ref(x, dft_seq_re, dft_seq_im, dft_hid_re, dft_hid_im):
        a = torch.einsum("bsd,ed->bse", x, dft_hid_re)
        b = torch.einsum("bsd,ed->bse", x, dft_hid_im)
        y = torch.einsum("ts,bse->bte", dft_seq_re, a) - torch.einsum("ts,bse->bte", dft_seq_im, b)
        return y

    x = A_x
    g = _sigmoid((x * gate_w).sum(-1, keepdims=True))
    a = _A_ref(x, A_wq, A_wk, A_wv, A_wo)
    b = _B_ref(x, B_dft_seq_re, B_dft_seq_im, B_dft_hid_re, B_dft_hid_im)
    return g * a + (1 - g) * b
```

```cpp
#include <hip/hip_runtime.h>
#include <hip/hip_bf16.h>
#include <stdint.h>

typedef float f32x16 __attribute__((ext_vector_type(16)));
typedef float f32x4 __attribute__((ext_vector_type(4)));
typedef float f32x2 __attribute__((ext_vector_type(2)));
typedef __bf16 bf16x8 __attribute__((ext_vector_type(8)));
typedef __bf16 bf16x2 __attribute__((ext_vector_type(2)));
typedef short s16x4 __attribute__((ext_vector_type(4)));
typedef unsigned u32x4 __attribute__((ext_vector_type(4)));
typedef unsigned u32x2 __attribute__((ext_vector_type(2)));
typedef unsigned short u16;
#define DI __device__ __forceinline__
#define LAS __attribute__((address_space(3)))
typedef LAS const char* lds_cptr;
typedef LAS char* lds_ptr;

constexpr int SEQ = 2048, DM = 640, NH = 32, DH = 20;
constexpr int ROWB = DM * 2;

constexpr int KV_STG = 7168;
constexpr size_t WS_Q    = 0;
constexpr size_t WS_KV   = WS_Q + (size_t)SEQ * ROWB;
constexpr size_t WS_KMAX = WS_KV + 64 * 4096;
constexpr size_t WS_GATE = WS_KMAX + 256;
constexpr size_t WS_AB   = WS_GATE + SEQ * 4;
constexpr size_t WS_WO   = WS_AB + (size_t)20 * 256 * 1024;
constexpr size_t WS_O    = WS_WO + (size_t)40 * 20 * 1024;
constexpr size_t WS_Y    = WS_O + (size_t)128 * 20 * 1024;
constexpr size_t WS_CNT  = WS_Y + (size_t)4 * SEQ * ROWB;
constexpr size_t WS_END  = WS_CNT + 96 * 128;

#define MFMA32(a, b, c) __builtin_amdgcn_mfma_f32_32x32x16_bf16((a), (b), (c), 0, 0, 0)
#define MFMA16(a, b, c) __builtin_amdgcn_mfma_f32_16x16x32_bf16((a), (b), (c), 0, 0, 0)
#define SBAR() __builtin_amdgcn_sched_barrier(0)

DI unsigned cvtpk(float lo, float hi) { f32x2 v = {lo, hi}; bf16x2 b = __builtin_convertvector(v, bf16x2); return __builtin_bit_cast(unsigned, b); }
DI float bflo(unsigned u) { return __uint_as_float(u << 16); }
DI float bfhi(unsigned u) { return __uint_as_float(u & 0xffff0000u); }
DI bf16x8 asfrag(u32x4 v) { return __builtin_bit_cast(bf16x8, v); }
DI bf16x8 ldsfrag(lds_cptr p) { return *(const LAS bf16x8*)p; }
DI s16x4 ldstr(lds_cptr p) { return __builtin_bit_cast(s16x4, __builtin_amdgcn_ds_read_tr16_b64_v4i16((LAS s16x4*)p)); }
DI bf16x8 trfrag(lds_cptr p0, lds_cptr p1) { s16x4 a = ldstr(p0), b = ldstr(p1); return __builtin_bit_cast(bf16x8, __builtin_shufflevector(a, b, 0, 1, 2, 3, 4, 5, 6, 7)); }
DI unsigned rfl(unsigned x) { return __builtin_amdgcn_readfirstlane(x); }
DI const char* uni(const void* p) {
    const unsigned long long a = (unsigned long long)p;
    return (const char*)(((unsigned long long)rfl((unsigned)(a >> 32)) << 32) | rfl((unsigned)a));
}
template <int N> DI void glds(const void* sbase_, unsigned voff, unsigned lds_) {
    unsigned keep;
    const char* sbase = uni(sbase_); const unsigned lds = rfl(lds_);
    if constexpr (N == 1)
        asm volatile("s_mov_b32 %0, m0\n\ts_mov_b32 m0, %3\n\ts_nop 3\n\tglobal_load_lds_dwordx4 %1, %2\n\ts_mov_b32 m0, %0" : "=&s"(keep) : "v"(voff), "s"(sbase), "s"(lds) : "memory");
    else if constexpr (N == 2)
        asm volatile("s_mov_b32 %0, m0\n\ts_mov_b32 m0, %3\n\ts_nop 3\n\tglobal_load_lds_dwordx4 %1, %2\n\tglobal_load_lds_dwordx4 %1, %2 offset:1024\n\ts_mov_b32 m0, %0" : "=&s"(keep) : "v"(voff), "s"(sbase), "s"(lds) : "memory");
    else if constexpr (N == 3)
        asm volatile("s_mov_b32 %0, m0\n\ts_mov_b32 m0, %3\n\ts_nop 3\n\tglobal_load_lds_dwordx4 %1, %2\n\tglobal_load_lds_dwordx4 %1, %2 offset:1024\n\tglobal_load_lds_dwordx4 %1, %2 offset:2048\n\ts_mov_b32 m0, %0" : "=&s"(keep) : "v"(voff), "s"(sbase), "s"(lds) : "memory");
    else
        asm volatile("s_mov_b32 %0, m0\n\ts_mov_b32 m0, %3\n\ts_nop 3\n\tglobal_load_lds_dwordx4 %1, %2\n\tglobal_load_lds_dwordx4 %1, %2 offset:1024\n\tglobal_load_lds_dwordx4 %1, %2 offset:2048\n\tglobal_load_lds_dwordx4 %1, %2 offset:3072\n\ts_mov_b32 m0, %0" : "=&s"(keep) : "v"(voff), "s"(sbase), "s"(lds) : "memory");
}
template <int N> DI void wait_vm_bar() { asm volatile("s_waitcnt vmcnt(%0)\n\ts_barrier" :: "i"(N) : "memory"); }
typedef __attribute__((address_space(1))) unsigned gu32;
DI __amdgpu_buffer_rsrc_t mk_rsrc(const void* p, unsigned bytes) { return __builtin_amdgcn_make_buffer_rsrc((void*)uni(p), 0, bytes, 0x00020000); }
DI void signal(char* ws, int idx) { __hip_atomic_fetch_add((gu32*)(ws + WS_CNT + idx * 128), 1u, __ATOMIC_RELAXED, __HIP_MEMORY_SCOPE_AGENT); }
DI void await(char* ws, int idx, unsigned n) {
    gu32* p = (gu32*)(uni(ws) + WS_CNT + idx * 128);
    for (int it = 0; it < (1 << 20); ++it) {
        if (rfl(__hip_atomic_load(p, __ATOMIC_RELAXED, __HIP_MEMORY_SCOPE_AGENT)) >= n) break;
        __builtin_amdgcn_s_sleep(1);
    }
    asm volatile("" ::: "memory");
}
DI void st16(void* p, u32x4 v) { *(u32x4*)p = v; }
DI void lds_bar() { asm volatile("s_waitcnt lgkmcnt(0)\n\ts_barrier" ::: "memory"); }
DI void swap32(unsigned& a, unsigned& b) { auto r = __builtin_amdgcn_permlane32_swap(a, b, false, false); a = r[0]; b = r[1]; }
DI float xhalf(float v) { unsigned a = __float_as_uint(v), b = a; swap32(a, b); return (threadIdx.x & 32) ? __uint_as_float(a) : __uint_as_float(b); }

constexpr int K1_NS = 5, K1_LDS = 163840;
constexpr unsigned K1_WIMG = 81920u;
template <int TYPE> DI void k1_loader(char* lds, const u16* xrows, const u16* wsrc, int g) {
    const int lane = threadIdx.x & 63;
    const unsigned lds0 = (unsigned)(uintptr_t)lds;
    constexpr unsigned STG = TYPE == 1 ? 16384u : 32768u;
    constexpr int NP = TYPE == 1 ? 4 : 8;
    unsigned voffA[4], voffB[4];
#pragma unroll
    for (int i = 0; i < 4; ++i) {
        const int p = g * 4 + i;
        { const int row = 8 * p + (lane >> 3), c = (lane & 7) ^ ((row >> 1) & 7); voffA[i] = row * ROWB + c * 16; }
        if (TYPE == 2) voffB[i] = voffA[i];
        if (TYPE == 0) { const int row = 4 * p + (lane >> 4), ch = (lane & 15) ^ (((row & 3) << 2) | ((row >> 2) & 3)); voffB[i] = row * ROWB + ch * 16; }
    }
    const char* abase = (const char*)xrows;
    const char* bbase = (const char*)wsrc;
    auto issue = [&](int ks) {
        const unsigned slot = lds0 + (ks % K1_NS) * STG + g * 4096;
#pragma unroll
        for (int i = 0; i < 4; ++i) {
            glds<1>(abase + ks * 128, voffA[i], slot + i * 1024);
            if (TYPE != 1) glds<1>(bbase + (TYPE == 0 ? ks * 64 * ROWB : ks * 128), voffB[i], slot + 16384 + i * 1024);
        }
    };
    issue(0); issue(1);
    wait_vm_bar<NP>();
    issue(2); issue(3); issue(4);
#pragma unroll
    for (int k = 0; k < 6; ++k) { wait_vm_bar<3 * NP>(); if (k + 5 < 10) issue(k + 5); }
    wait_vm_bar<2 * NP>();
    wait_vm_bar<NP>();
    wait_vm_bar<0>();
}

template <int TYPE>
DI void k1_body(char* lds, const u16* wk, const u16* wv, const u16* gw, char* ws, int mt, int nt) {
    const int tid = threadIdx.x, lane = tid & 63, r32 = lane & 31, hi = lane >> 5;
    const int wid = rfl(tid >> 6), wm = wid >> 1, wn = wid & 1;
    constexpr unsigned STG = TYPE == 1 ? 16384u : 32768u;
    if (TYPE == 1) {
        const int c = tid & 7, cq = c & 3;
        const u16* wsrc = (c < 4 ? wk : wv) + min(cq, 2) * 8;
        u32x2 lo[20], hi2[20]; unsigned gv[20];
#pragma unroll
        for (int it = 0; it < 20; ++it) {
            const int k = it * 32 + (tid >> 3);
            lo[it] = *(const u32x2*)(wsrc + k * 20);
            hi2[it] = *(const u32x2*)(wsrc + k * 20 + (cq < 2 ? 4 : 0));
            gv[it] = gw[k];
        }
        SBAR();
#pragma unroll
        for (int it = 0; it < 20; ++it) {
            const int k = it * 32 + (tid >> 3);
            u32x4 v = {lo[it][0], lo[it][1], hi2[it][0], hi2[it][1]};
            if (cq == 2) { v[2] = c == 6 ? gv[it] : 0u; v[3] = 0u; }
            if (cq == 3) v = (u32x4){0, 0, 0, 0};
            *(LAS u32x4*)((lds_ptr)lds + K1_WIMG + k * 128 + c * 16) = v;
        }
    }
    lds_cptr xa[4];
#pragma unroll
    for (int ss = 0; ss < 4; ++ss) xa[ss] = (lds_cptr)lds + r32 * 128 + (((2 * ss + hi) ^ ((r32 >> 1) & 7)) * 16);
    const int blk = (lane >> 4) & 1, q4 = (lane & 15) >> 2, p4 = lane & 3;
    lds_cptr wa[2][2];
    if (TYPE == 0) {
#pragma unroll
        for (int nb = 0; nb < 2; ++nb)
#pragma unroll
            for (int t = 0; t < 2; ++t) {
                const int row = 8 * hi + 4 * t + q4, ch = 4 * (2 * wn + nb) + 2 * blk + (p4 >> 1);
                wa[nb][t] = (lds_cptr)lds + 16384 + 256 * row + 16 * (ch ^ (((row & 3) << 2) | ((row >> 2) & 3))) + 8 * (p4 & 1);
            }
    }
    lds_cptr kva = (lds_cptr)lds + K1_WIMG + (8 * hi + q4) * 128 + blk * 32 + p4 * 8;
    f32x16 acc[2][2];
#pragma unroll
    for (int i = 0; i < 2; ++i)
#pragma unroll
        for (int j = 0; j < 2; ++j) acc[i][j] = (f32x16){};
    bf16x8 xf[2][2], wf[2][2];
    auto loadf = [&](int buf, int ks, int ss) {
        const unsigned so = (ks % K1_NS) * STG;
        if (TYPE == 1) {
            xf[buf][0] = ldsfrag(xa[ss] + so + wid * 4096);
            lds_cptr b = kva + (ks * 64 + ss * 16) * 128;
            wf[buf][0] = trfrag(b, b + 512); wf[buf][1] = trfrag(b + 64, b + 64 + 512);
        } else {
#pragma unroll
            for (int i = 0; i < 2; ++i) xf[buf][i] = ldsfrag(xa[ss] + so + (2 * wm + i) * 4096);
#pragma unroll
            for (int j = 0; j < 2; ++j) {
                if (TYPE == 0) wf[buf][j] = trfrag(wa[j][0] + so + ss * 4096, wa[j][1] + so + ss * 4096);
                else wf[buf][j] = ldsfrag(xa[ss] + so + 16384 + (2 * wn + j) * 4096);
            }
        }
    };
    lds_bar();
    loadf(0, 0, 0);
#pragma unroll
    for (int ks = 0; ks < 10; ++ks) {
#pragma unroll
        for (int ss = 0; ss < 4; ++ss) {
            const int cur = ss & 1, nxt = cur ^ 1;
            if (ss < 3) loadf(nxt, ks, ss + 1);
            else if (ks + 1 < 10) { lds_bar(); loadf(nxt, ks + 1, 0); }
            SBAR();
            if (TYPE == 1) {
                acc[0][0] = MFMA32(wf[cur][0], xf[cur][0], acc[0][0]);
                acc[0][1] = MFMA32(xf[cur][0], wf[cur][1], acc[0][1]);
            } else {
#pragma unroll
                for (int i = 0; i < 2; ++i)
#pragma unroll
                    for (int j = 0; j < 2; ++j) {
                        if (TYPE == 0) acc[i][j] = MFMA32(wf[cur][j], xf[cur][i], acc[i][j]);
                        else acc[i][j] = MFMA32(xf[cur][i], wf[cur][j], acc[i][j]);
                    }
            }
            SBAR();
        }
    }
    if (TYPE == 0) {
        const float sc = 0.22360679774997896f * 1.4426950408889634f;
        lds_bar();
#pragma unroll
        for (int i = 0; i < 2; ++i)
#pragma unroll
            for (int j = 0; j < 2; ++j) {
                const int tl = (2 * wm + i) * 32 + r32;
#pragma unroll
                for (int gp = 0; gp < 2; ++gp) {
                    const f32x16& a = acc[i][j];
                    unsigned x0 = cvtpk(a[8 * gp + 0] * sc, a[8 * gp + 1] * sc), x1 = cvtpk(a[8 * gp + 2] * sc, a[8 * gp + 3] * sc);
                    unsigned y0 = cvtpk(a[8 * gp + 4] * sc, a[8 * gp + 5] * sc), y1 = cvtpk(a[8 * gp + 6] * sc, a[8 * gp + 7] * sc);
                    swap32(x0, y0); swap32(x1, y1);
                    const int ch = (2 * wn + j) * 4 + 2 * gp + hi;
                    *(LAS u32x4*)((lds_ptr)lds + tl * 256 + ((ch ^ (tl & 15)) * 16)) = (u32x4){x0, x1, y0, y1};
                }
            }
        lds_bar();
        char* qb = ws + WS_Q + (size_t)mt * 128 * ROWB + nt * 256;
#pragma unroll
        for (int it = 0; it < 8; ++it) {
            const int tl = it * 16 + (tid >> 4), ch = tid & 15;
            const u32x4 v = *(const LAS u32x4*)((lds_ptr)lds + tl * 256 + ((ch ^ (tl & 15)) * 16));
            st16(qb + tl * ROWB + ch * 16, v);
        }
    } else if (TYPE == 2) {
        const int half = nt >= 5; const float sg = half ? -1.f : 1.f;
#pragma unroll
        for (int i = 0; i < 2; ++i)
#pragma unroll
            for (int j = 0; j < 2; ++j) {
                const int s0 = mt * 128 + (2 * wm + i) * 32, nblk = (nt - 5 * half) * 4 + 2 * wn + j;
                char* dst = ws + WS_AB + ((size_t)(nblk * 256 + half * 128 + (s0 >> 4)) * 1024) + lane * 16;
#pragma unroll
                for (int gp = 0; gp < 2; ++gp) {
                    const f32x16& a = acc[i][j];
                    unsigned x0 = cvtpk(a[8 * gp + 0] * sg, a[8 * gp + 1] * sg), x1 = cvtpk(a[8 * gp + 2] * sg, a[8 * gp + 3] * sg);
                    unsigned y0 = cvtpk(a[8 * gp + 4] * sg, a[8 * gp + 5] * sg), y1 = cvtpk(a[8 * gp + 6] * sg, a[8 * gp + 7] * sg);
                    swap32(x0, y0); swap32(x1, y1);
                    st16(dst + gp * 1024, (u32x4){x0, x1, y0, y1});
                }
            }
    } else {
        const int kt = mt * 4 + wid, kh = kt >> 5;
        char* dst = ws + WS_KV + (kt & 31) * KV_STG;
        const f32x16& k = acc[0][0]; const f32x16& v = acc[0][1];
        unsigned pk[8];
#pragma unroll
        for (int i = 0; i < 8; ++i) pk[i] = cvtpk(k[2 * i], k[2 * i + 1]);
        float n2 = 0.f;
#pragma unroll
        for (int i = 0; i < 6; ++i) { const float a = bflo(pk[i]), b = bfhi(pk[i]); n2 += a * a + b * b; }
        n2 += xhalf(n2);
        swap32(pk[0], pk[2]); swap32(pk[1], pk[3]); swap32(pk[4], pk[6]); swap32(pk[5], pk[7]);
        st16(dst + kh * 1024 + lane * 16, (u32x4){pk[0], pk[1], pk[2], pk[3]});
        if (!hi) *(u32x4*)(dst + 2048 + kh * 512 + lane * 16) = (u32x4){pk[4], pk[5], 0x00003F80u, 0u};
        if (r32 == 20) {
            float* g = (float*)(ws + WS_GATE) + kt * 32;
#pragma unroll
            for (int i = 0; i < 16; ++i) g[(i & 3) + 8 * (i >> 2) + 4 * hi] = 1.f / (1.f + __expf(-v[i]));
        }
#pragma unroll
        for (int s = 0; s < 2; ++s) {
            u32x4 f = {cvtpk(v[8 * s], v[8 * s + 1]), cvtpk(v[8 * s + 2], v[8 * s + 3]), cvtpk(v[8 * s + 4], v[8 * s + 5]), cvtpk(v[8 * s + 6], v[8 * s + 7])};
            if (r32 == 20 || r32 == 24) f = (u32x4){0x3F803F80u, 0x3F803F80u, 0x3F803F80u, 0x3F803F80u};
            else if (r32 > 20) f = (u32x4){0, 0, 0, 0};
            st16(dst + 3072 + kh * 2048 + s * 1024 + lane * 16, f);
        }
#pragma unroll
        for (int o = 16; o >= 1; o >>= 1) n2 = fmaxf(n2, __shfl_xor(n2, o));
        lds_bar();
        LAS float* red = (LAS float*)lds;
        if (lane == 0) red[wid] = n2;
        lds_bar();
        if (tid == 0) ((float*)(ws + WS_KMAX))[mt] = fmaxf(fmaxf(red[0], red[1]), fmaxf(red[2], red[3]));
    }
}

__global__ __launch_bounds__(512, 1) void k1_proj(const u16* xA, const u16* wq, const u16* wk, const u16* wv, const u16* wo,
                                                  const u16* hre, const u16* him, const u16* gw, char* ws) {
    extern __shared__ __attribute__((aligned(16))) char lds[];
    const int b = blockIdx.x, xcd = b & 7, j = b >> 3, mt = (xcd >> 1) * 4 + (j >> 3), sub = (xcd & 1) * 8 + (j & 7);
    const int wid = rfl(threadIdx.x >> 6);
    if (b == 255 && threadIdx.x < 96) *(unsigned*)(ws + WS_CNT + threadIdx.x * 128) = 0u;
    const u16* xr = xA + mt * 128 * DM;
    if (wid >= 4) {
        if (sub < 5) k1_loader<0>(lds, xr, wq + sub * 128, wid - 4);
        else if (sub == 5) k1_loader<1>(lds, xr, wq, wid - 4);
        else { const int nt = sub - 6; k1_loader<2>(lds, xr, (nt < 5 ? hre + nt * 128 * DM : him + (nt - 5) * 128 * DM), wid - 4); }
        const int f = b * 4 + wid - 4;
        if (f < 800) {
            const int lane = threadIdx.x & 63, nb = f / 20, ks = f % 20;
            const u16* src = wo + (size_t)(32 * ks + 8 * (lane >> 4)) * DM + 16 * nb + (lane & 15);
            unsigned e[8];
#pragma unroll
            for (int jj = 0; jj < 8; ++jj) e[jj] = src[jj * DM];
            st16(ws + WS_WO + (size_t)f * 1024 + lane * 16, (u32x4){e[0] | (e[1] << 16), e[2] | (e[3] << 16), e[4] | (e[5] << 16), e[6] | (e[7] << 16)});
        }
    } else {
        if (sub < 5) k1_body<0>(lds, wk, wv, gw, ws, mt, sub);
        else if (sub == 5) k1_body<1>(lds, wk, wv, gw, ws, mt, 0);
        else k1_body<2>(lds, wk, wv, gw, ws, mt, sub - 6);
    }
}

constexpr int A_BASE = 5 * KV_STG, A_STG = 16384, B_BASE = A_BASE + 4 * A_STG, NBS = 12, FLAG_BASE = B_BASE + 5 * NBS * 1024, K2_LDS = FLAG_BASE + 64, Q_BASE = A_BASE + 3 * A_STG;
constexpr int W_BASE = A_BASE, YS_BASE = A_BASE + 102400, P_ROW = 336;
constexpr int SPS = 3;
DI float EX2(float x) { return __builtin_amdgcn_exp2f(x); }

DI void attn_exact(const char* kv, bf16x8 q0, bf16x8 q1, f32x16& o, int lane) {
    u32x4 q1z = __builtin_bit_cast(u32x4, q1); q1z[2] = 0; q1z[3] = 0;
    const bf16x8 q1n = asfrag(q1z);
    float m = -3.0e38f; o = (f32x16){};
    for (int t = 0; t < 64; ++t) {
        const char* p = kv + (t & 31) * KV_STG; const int kh = t >> 5;
        const bf16x8 k0 = *(const bf16x8*)(p + kh * 1024 + lane * 16), k1 = *(const bf16x8*)(p + 2048 + kh * 512 + (lane & 31) * 16);
        const bf16x8 v0 = *(const bf16x8*)(p + 3072 + kh * 2048 + lane * 16), v1 = *(const bf16x8*)(p + 4096 + kh * 2048 + lane * 16);
        f32x16 s = MFMA32(k0, q0, (f32x16){}); s = MFMA32(k1, q1n, s);
        float mx = s[0];
#pragma unroll
        for (int i = 1; i < 16; ++i) mx = fmaxf(mx, s[i]);
        mx = fmaxf(mx, xhalf(mx));
        const float mn = fmaxf(m, mx), f = EX2(m - mn); m = mn;
#pragma unroll
        for (int i = 0; i < 16; ++i) { o[i] *= f; s[i] = EX2(s[i] - mn); }
        const u32x4 p0 = {cvtpk(s[0], s[1]), cvtpk(s[2], s[3]), cvtpk(s[4], s[5]), cvtpk(s[6], s[7])};
        const u32x4 p1 = {cvtpk(s[8], s[9]), cvtpk(s[10], s[11]), cvtpk(s[12], s[13]), cvtpk(s[14], s[15])};
        o = MFMA32(v0, asfrag(p0), o); o = MFMA32(v1, asfrag(p1), o);
    }
}

DI void attn_role(char* lds, char* ws, u16* out, int b) {
    const int tid = threadIdx.x, lane = tid & 63, r32 = lane & 31, hi = lane >> 5;
    const int wid = rfl(tid >> 6), pr = wid & 3, kh = wid >> 2;
    const int tl = 2 * pr + kh, tok = b * 8 + tl;
    const float gate = *(const float*)uni(ws + WS_GATE + tok * 4);
    unsigned kmu = 0;
    {
        const unsigned* kp = (const unsigned*)uni(ws + WS_KMAX);
#pragma unroll
        for (int i = 0; i < 16; ++i) kmu = max(kmu, kp[i]);
    }
    if (lane < 2) *(LAS unsigned*)((lds_ptr)lds + FLAG_BASE + (wid * 2 + lane) * 4) = 0u;
    lds_bar();
    u32x2 qa[2], qc[2], qe[2];
#pragma unroll
    for (int t = 0; t < 2; ++t) {
        lds_cptr qp = (lds_cptr)lds + Q_BASE + (2 * pr + t) * ROWB + r32 * 40;
        qa[t] = *(const LAS u32x2*)(qp + 16 * hi); qc[t] = *(const LAS u32x2*)(qp + 16 * hi + 8); qe[t] = *(const LAS u32x2*)(qp + 32);
    }
    const float km = __uint_as_float(kmu);
    bf16x8 q0[2], q1[2];
#pragma unroll
    for (int t = 0; t < 2; ++t) {
        const u32x2 a = qa[t], c = qc[t]; u32x2 ee = qe[t];
        if (hi) ee = (u32x2){0, 0};
        float n2 = 0.f;
        const unsigned w[6] = {a[0], a[1], c[0], c[1], ee[0], ee[1]};
#pragma unroll
        for (int i = 0; i < 6; ++i) { const float x = bflo(w[i]), y = bfhi(w[i]); n2 += x * x + y * y; }
        n2 += xhalf(n2);
        const float mref = __builtin_amdgcn_sqrtf(n2 * km) * 1.001f - 88.f;
        const unsigned mneg = cvtpk(-mref, 0.f) & 0xffffu;
        q0[t] = asfrag((u32x4){a[0], a[1], c[0], c[1]});
        q1[t] = asfrag((u32x4){ee[0], ee[1], hi ? 0u : mneg, 0u});
    }
    f32x16 o0, o1;
    {
        const unsigned l0 = (unsigned)(uintptr_t)lds;
        unsigned ak0 = l0 + kh * 1024 + lane * 16, ak1 = l0 + 2048 + kh * 512 + r32 * 16, av = l0 + 3072 + kh * 2048 + lane * 16;
        unsigned cnt;
        asm volatile(
    "v_mov_b32 v40, 0\n"
    "v_mov_b32 v41, 0\n"
    "v_mov_b32 v42, 0\n"
    "v_mov_b32 v43, 0\n"
    "v_mov_b32 v44, 0\n"
    "v_mov_b32 v45, 0\n"
    "v_mov_b32 v46, 0\n"
    "v_mov_b32 v47, 0\n"
    "v_mov_b32 v48, 0\n"
    "v_mov_b32 v49, 0\n"
    "v_mov_b32 v50, 0\n"
    "v_mov_b32 v51, 0\n"
    "v_mov_b32 v52, 0\n"
    "v_mov_b32 v53, 0\n"
    "v_mov_b32 v54, 0\n"
    "v_mov_b32 v55, 0\n"
    "v_mov_b32 v56, 0\n"
    "v_mov_b32 v57, 0\n"
    "v_mov_b32 v58, 0\n"
    "v_mov_b32 v59, 0\n"
    "v_mov_b32 v60, 0\n"
    "v_mov_b32 v61, 0\n"
    "v_mov_b32 v62, 0\n"
    "v_mov_b32 v63, 0\n"
    "v_mov_b32 v64, 0\n"
    "v_mov_b32 v65, 0\n"
    "v_mov_b32 v66, 0\n"
    "v_mov_b32 v67, 0\n"
    "v_mov_b32 v68, 0\n"
    "v_mov_b32 v69, 0\n"
    "v_mov_b32 v70, 0\n"
    "v_mov_b32 v71, 0\n"
    "v_mov_b32 v96, 0\n"
    "v_mov_b32 v97, 0\n"
    "v_mov_b32 v98, 0\n"
    "v_mov_b32 v99, 0\n"
    "v_mov_b32 v100, 0\n"
    "v_mov_b32 v101, 0\n"
    "v_mov_b32 v102, 0\n"
    "v_mov_b32 v103, 0\n"
    "v_mov_b32 v112, 0\n"
    "v_mov_b32 v113, 0\n"
    "v_mov_b32 v114, 0\n"
    "v_mov_b32 v115, 0\n"
    "v_mov_b32 v116, 0\n"
    "v_mov_b32 v117, 0\n"
    "v_mov_b32 v118, 0\n"
    "v_mov_b32 v119, 0\n"
    "s_mov_b32 %[cnt], 6\n"
    "s_barrier\n"
    "ds_read_b128 v[88:91], v120\n"
    "ds_read_b128 v[92:95], v121\n"
    "s_waitcnt lgkmcnt(1)\n"
    "v_mfma_f32_32x32x16_bf16 v[8:23], v[88:91], v[72:75], 0\n"
    "s_waitcnt lgkmcnt(0)\n"
    "v_mfma_f32_32x32x16_bf16 v[8:23], v[92:95], v[76:79], v[8:23]\n"
    "s_nop 7\n"
    "1:\n"
    "s_barrier\n"
    "v_mfma_f32_32x32x16_bf16 v[24:39], v[88:91], v[80:83], 0\n"
    "ds_read_b128 v[88:91], v120 offset:7168\n"
    "v_exp_f32 v8, v8\n"
    "v_exp_f32 v9, v9\n"
    "v_exp_f32 v10, v10\n"
    "v_exp_f32 v11, v11\n"
    "v_cvt_pk_bf16_f32 v104, v8, v9\n"
    "v_cvt_pk_bf16_f32 v105, v10, v11\n"
    "v_mfma_f32_32x32x16_bf16 v[56:71], v[96:99], v[112:115], v[56:71]\n"
    "ds_read_b128 v[96:99], v122 offset:0\n"
    "v_exp_f32 v12, v12\n"
    "v_exp_f32 v13, v13\n"
    "v_exp_f32 v14, v14\n"
    "v_exp_f32 v15, v15\n"
    "v_cvt_pk_bf16_f32 v106, v12, v13\n"
    "v_cvt_pk_bf16_f32 v107, v14, v15\n"
    "v_mfma_f32_32x32x16_bf16 v[24:39], v[92:95], v[84:87], v[24:39]\n"
    "ds_read_b128 v[92:95], v121 offset:7168\n"
    "v_exp_f32 v16, v16\n"
    "v_exp_f32 v17, v17\n"
    "v_exp_f32 v18, v18\n"
    "v_exp_f32 v19, v19\n"
    "v_cvt_pk_bf16_f32 v108, v16, v17\n"
    "v_cvt_pk_bf16_f32 v109, v18, v19\n"
    "v_mfma_f32_32x32x16_bf16 v[56:71], v[100:103], v[116:119], v[56:71]\n"
    "ds_read_b128 v[100:103], v122 offset:1024\n"
    "v_exp_f32 v20, v20\n"
    "v_exp_f32 v21, v21\n"
    "v_exp_f32 v22, v22\n"
    "v_exp_f32 v23, v23\n"
    "v_cvt_pk_bf16_f32 v110, v20, v21\n"
    "v_cvt_pk_bf16_f32 v111, v22, v23\n"
    "s_waitcnt lgkmcnt(3)\n"
    "v_mfma_f32_32x32x16_bf16 v[8:23], v[88:91], v[72:75], 0\n"
    "v_exp_f32 v24, v24\n"
    "v_exp_f32 v25, v25\n"
    "v_exp_f32 v26, v26\n"
    "v_exp_f32 v27, v27\n"
    "v_cvt_pk_bf16_f32 v112, v24, v25\n"
    "v_cvt_pk_bf16_f32 v113, v26, v27\n"
    "s_waitcnt lgkmcnt(2)\n"
    "v_mfma_f32_32x32x16_bf16 v[40:55], v[96:99], v[104:107], v[40:55]\n"
    "v_exp_f32 v28, v28\n"
    "v_exp_f32 v29, v29\n"
    "v_exp_f32 v30, v30\n"
    "v_exp_f32 v31, v31\n"
    "v_cvt_pk_bf16_f32 v114, v28, v29\n"
    "v_cvt_pk_bf16_f32 v115, v30, v31\n"
    "s_waitcnt lgkmcnt(1)\n"
    "v_mfma_f32_32x32x16_bf16 v[8:23], v[92:95], v[76:79], v[8:23]\n"
    "v_exp_f32 v32, v32\n"
    "v_exp_f32 v33, v33\n"
    "v_exp_f32 v34, v34\n"
    "v_exp_f32 v35, v35\n"
    "v_cvt_pk_bf16_f32 v116, v32, v33\n"
    "v_cvt_pk_bf16_f32 v117, v34, v35\n"
    "s_waitcnt lgkmcnt(0)\n"
    "v_mfma_f32_32x32x16_bf16 v[40:55], v[100:103], v[108:111], v[40:55]\n"
    "v_exp_f32 v36, v36\n"
    "v_exp_f32 v37, v37\n"
    "v_exp_f32 v38, v38\n"
    "v_exp_f32 v39, v39\n"
    "v_cvt_pk_bf16_f32 v118, v36, v37\n"
    "v_cvt_pk_bf16_f32 v119, v38, v39\n"
    "s_barrier\n"
    "v_mfma_f32_32x32x16_bf16 v[24:39], v[88:91], v[80:83], 0\n"
    "ds_read_b128 v[88:91], v120 offset:14336\n"
    "v_exp_f32 v8, v8\n"
    "v_exp_f32 v9, v9\n"
    "v_exp_f32 v10, v10\n"
    "v_exp_f32 v11, v11\n"
    "v_cvt_pk_bf16_f32 v104, v8, v9\n"
    "v_cvt_pk_bf16_f32 v105, v10, v11\n"
    "v_mfma_f32_32x32x16_bf16 v[56:71], v[96:99], v[112:115], v[56:71]\n"
    "ds_read_b128 v[96:99], v122 offset:7168\n"
    "v_exp_f32 v12, v12\n"
    "v_exp_f32 v13, v13\n"
    "v_exp_f32 v14, v14\n"
    "v_exp_f32 v15, v15\n"
    "v_cvt_pk_bf16_f32 v106, v12, v13\n"
    "v_cvt_pk_bf16_f32 v107, v14, v15\n"
    "v_mfma_f32_32x32x16_bf16 v[24:39], v[92:95], v[84:87], v[24:39]\n"
    "ds_read_b128 v[92:95], v121 offset:14336\n"
    "v_exp_f32 v16, v16\n"
    "v_exp_f32 v17, v17\n"
    "v_exp_f32 v18, v18\n"
    "v_exp_f32 v19, v19\n"
    "v_cvt_pk_bf16_f32 v108, v16, v17\n"
    "v_cvt_pk_bf16_f32 v109, v18, v19\n"
    "v_mfma_f32_32x32x16_bf16 v[56:71], v[100:103], v[116:119], v[56:71]\n"
    "ds_read_b128 v[100:103], v122 offset:8192\n"
    "v_exp_f32 v20, v20\n"
    "v_exp_f32 v21, v21\n"
    "v_exp_f32 v22, v22\n"
    "v_exp_f32 v23, v23\n"
    "v_cvt_pk_bf16_f32 v110, v20, v21\n"
    "v_cvt_pk_bf16_f32 v111, v22, v23\n"
    "s_waitcnt lgkmcnt(3)\n"
    "v_mfma_f32_32x32x16_bf16 v[8:23], v[88:91], v[72:75], 0\n"
    "v_exp_f32 v24, v24\n"
    "v_exp_f32 v25, v25\n"
    "v_exp_f32 v26, v26\n"
    "v_exp_f32 v27, v27\n"
    "v_cvt_pk_bf16_f32 v112, v24, v25\n"
    "v_cvt_pk_bf16_f32 v113, v26, v27\n"
    "s_waitcnt lgkmcnt(2)\n"
    "v_mfma_f32_32x32x16_bf16 v[40:55], v[96:99], v[104:107], v[40:55]\n"
    "v_exp_f32 v28, v28\n"
    "v_exp_f32 v29, v29\n"
    "v_exp_f32 v30, v30\n"
    "v_exp_f32 v31, v31\n"
    "v_cvt_pk_bf16_f32 v114, v28, v29\n"
    "v_cvt_pk_bf16_f32 v115, v30, v31\n"
    "s_waitcnt lgkmcnt(1)\n"
    "v_mfma_f32_32x32x16_bf16 v[8:23], v[92:95], v[76:79], v[8:23]\n"
    "v_exp_f32 v32, v32\n"
    "v_exp_f32 v33, v33\n"
    "v_exp_f32 v34, v34\n"
    "v_exp_f32 v35, v35\n"
    "v_cvt_pk_bf16_f32 v116, v32, v33\n"
    "v_cvt_pk_bf16_f32 v117, v34, v35\n"
    "s_waitcnt lgkmcnt(0)\n"
    "v_mfma_f32_32x32x16_bf16 v[40:55], v[100:103], v[108:111], v[40:55]\n"
    "v_exp_f32 v36, v36\n"
    "v_exp_f32 v37, v37\n"
    "v_exp_f32 v38, v38\n"
    "v_exp_f32 v39, v39\n"
    "v_cvt_pk_bf16_f32 v118, v36, v37\n"
    "v_cvt_pk_bf16_f32 v119, v38, v39\n"
    "s_barrier\n"
    "v_mfma_f32_32x32x16_bf16 v[24:39], v[88:91], v[80:83], 0\n"
    "ds_read_b128 v[88:91], v120 offset:21504\n"
    "v_exp_f32 v8, v8\n"
    "v_exp_f32 v9, v9\n"
    "v_exp_f32 v10, v10\n"
    "v_exp_f32 v11, v11\n"
    "v_cvt_pk_bf16_f32 v104, v8, v9\n"
    "v_cvt_pk_bf16_f32 v105, v10, v11\n"
    "v_mfma_f32_32x32x16_bf16 v[56:71], v[96:99], v[112:115], v[56:71]\n"
    "ds_read_b128 v[96:99], v122 offset:14336\n"
    "v_exp_f32 v12, v12\n"
    "v_exp_f32 v13, v13\n"
    "v_exp_f32 v14, v14\n"
    "v_exp_f32 v15, v15\n"
    "v_cvt_pk_bf16_f32 v106, v12, v13\n"
    "v_cvt_pk_bf16_f32 v107, v14, v15\n"
    "v_mfma_f32_32x32x16_bf16 v[24:39], v[92:95], v[84:87], v[24:39]\n"
    "ds_read_b128 v[92:95], v121 offset:21504\n"
    "v_exp_f32 v16, v16\n"
    "v_exp_f32 v17, v17\n"
    "v_exp_f32 v18, v18\n"
    "v_exp_f32 v19, v19\n"
    "v_cvt_pk_bf16_f32 v108, v16, v17\n"
    "v_cvt_pk_bf16_f32 v109, v18, v19\n"
    "v_mfma_f32_32x32x16_bf16 v[56:71], v[100:103], v[116:119], v[56:71]\n"
    "ds_read_b128 v[100:103], v122 offset:15360\n"
    "v_exp_f32 v20, v20\n"
    "v_exp_f32 v21, v21\n"
    "v_exp_f32 v22, v22\n"
    "v_exp_f32 v23, v23\n"
    "v_cvt_pk_bf16_f32 v110, v20, v21\n"
    "v_cvt_pk_bf16_f32 v111, v22, v23\n"
    "s_waitcnt lgkmcnt(3)\n"
    "v_mfma_f32_32x32x16_bf16 v[8:23], v[88:91], v[72:75], 0\n"
    "v_exp_f32 v24, v24\n"
    "v_exp_f32 v25, v25\n"
    "v_exp_f32 v26, v26\n"
    "v_exp_f32 v27, v27\n"
    "v_cvt_pk_bf16_f32 v112, v24, v25\n"
    "v_cvt_pk_bf16_f32 v113, v26, v27\n"
    "s_waitcnt lgkmcnt(2)\n"
    "v_mfma_f32_32x32x16_bf16 v[40:55], v[96:99], v[104:107], v[40:55]\n"
    "v_exp_f32 v28, v28\n"
    "v_exp_f32 v29, v29\n"
    "v_exp_f32 v30, v30\n"
    "v_exp_f32 v31, v31\n"
    "v_cvt_pk_bf16_f32 v114, v28, v29\n"
    "v_cvt_pk_bf16_f32 v115, v30, v31\n"
    "s_waitcnt lgkmcnt(1)\n"
    "v_mfma_f32_32x32x16_bf16 v[8:23], v[92:95], v[76:79], v[8:23]\n"
    "v_exp_f32 v32, v32\n"
    "v_exp_f32 v33, v33\n"
    "v_exp_f32 v34, v34\n"
    "v_exp_f32 v35, v35\n"
    "v_cvt_pk_bf16_f32 v116, v32, v33\n"
    "v_cvt_pk_bf16_f32 v117, v34, v35\n"
    "s_waitcnt lgkmcnt(0)\n"
    "v_mfma_f32_32x32x16_bf16 v[40:55], v[100:103], v[108:111], v[40:55]\n"
    "v_exp_f32 v36, v36\n"
    "v_exp_f32 v37, v37\n"
    "v_exp_f32 v38, v38\n"
    "v_exp_f32 v39, v39\n"
    "v_cvt_pk_bf16_f32 v118, v36, v37\n"
    "v_cvt_pk_bf16_f32 v119, v38, v39\n"
    "s_barrier\n"
    "v_mfma_f32_32x32x16_bf16 v[24:39], v[88:91], v[80:83], 0\n"
    "ds_read_b128 v[88:91], v120 offset:28672\n"
    "v_exp_f32 v8, v8\n"
    "v_exp_f32 v9, v9\n"
    "v_exp_f32 v10, v10\n"
    "v_exp_f32 v11, v11\n"
    "v_cvt_pk_bf16_f32 v104, v8, v9\n"
    "v_cvt_pk_bf16_f32 v105, v10, v11\n"
    "v_mfma_f32_32x32x16_bf16 v[56:71], v[96:99], v[112:115], v[56:71]\n"
    "ds_read_b128 v[96:99], v122 offset:21504\n"
    "v_exp_f32 v12, v12\n"
    "v_exp_f32 v13, v13\n"
    "v_exp_f32 v14, v14\n"
    "v_exp_f32 v15, v15\n"
    "v_cvt_pk_bf16_f32 v106, v12, v13\n"
    "v_cvt_pk_bf16_f32 v107, v14, v15\n"
    "v_mfma_f32_32x32x16_bf16 v[24:39], v[92:95], v[84:87], v[24:39]\n"
    "ds_read_b128 v[92:95], v121 offset:28672\n"
    "v_exp_f32 v16, v16\n"
    "v_exp_f32 v17, v17\n"
    "v_exp_f32 v18, v18\n"
    "v_exp_f32 v19, v19\n"
    "v_cvt_pk_bf16_f32 v108, v16, v17\n"
    "v_cvt_pk_bf16_f32 v109, v18, v19\n"
    "v_mfma_f32_32x32x16_bf16 v[56:71], v[100:103], v[116:119], v[56:71]\n"
    "ds_read_b128 v[100:103], v122 offset:22528\n"
    "v_exp_f32 v20, v20\n"
    "v_exp_f32 v21, v21\n"
    "v_exp_f32 v22, v22\n"
    "v_exp_f32 v23, v23\n"
    "v_cvt_pk_bf16_f32 v110, v20, v21\n"
    "v_cvt_pk_bf16_f32 v111, v22, v23\n"
    "s_waitcnt lgkmcnt(3)\n"
    "v_mfma_f32_32x32x16_bf16 v[8:23], v[88:91], v[72:75], 0\n"
    "v_exp_f32 v24, v24\n"
    "v_exp_f32 v25, v25\n"
    "v_exp_f32 v26, v26\n"
    "v_exp_f32 v27, v27\n"
    "v_cvt_pk_bf16_f32 v112, v24, v25\n"
    "v_cvt_pk_bf16_f32 v113, v26, v27\n"
    "s_waitcnt lgkmcnt(2)\n"
    "v_mfma_f32_32x32x16_bf16 v[40:55], v[96:99], v[104:107], v[40:55]\n"
    "v_exp_f32 v28, v28\n"
    "v_exp_f32 v29, v29\n"
    "v_exp_f32 v30, v30\n"
    "v_exp_f32 v31, v31\n"
    "v_cvt_pk_bf16_f32 v114, v28, v29\n"
    "v_cvt_pk_bf16_f32 v115, v30, v31\n"
    "s_waitcnt lgkmcnt(1)\n"
    "v_mfma_f32_32x32x16_bf16 v[8:23], v[92:95], v[76:79], v[8:23]\n"
    "v_exp_f32 v32, v32\n"
    "v_exp_f32 v33, v33\n"
    "v_exp_f32 v34, v34\n"
    "v_exp_f32 v35, v35\n"
    "v_cvt_pk_bf16_f32 v116, v32, v33\n"
    "v_cvt_pk_bf16_f32 v117, v34, v35\n"
    "s_waitcnt lgkmcnt(0)\n"
    "v_mfma_f32_32x32x16_bf16 v[40:55], v[100:103], v[108:111], v[40:55]\n"
    "v_exp_f32 v36, v36\n"
    "v_exp_f32 v37, v37\n"
    "v_exp_f32 v38, v38\n"
    "v_exp_f32 v39, v39\n"
    "v_cvt_pk_bf16_f32 v118, v36, v37\n"
    "v_cvt_pk_bf16_f32 v119, v38, v39\n"
    "s_barrier\n"
    "v_mfma_f32_32x32x16_bf16 v[24:39], v[88:91], v[80:83], 0\n"
    "ds_read_b128 v[88:91], v120 offset:0\n"
    "v_exp_f32 v8, v8\n"
    "v_exp_f32 v9, v9\n"
    "v_exp_f32 v10, v10\n"
    "v_exp_f32 v11, v11\n"
    "v_cvt_pk_bf16_f32 v104, v8, v9\n"
    "v_cvt_pk_bf16_f32 v105, v10, v11\n"
    "v_mfma_f32_32x32x16_bf16 v[56:71], v[96:99], v[112:115], v[56:71]\n"
    "ds_read_b128 v[96:99], v122 offset:28672\n"
    "v_exp_f32 v12, v12\n"
    "v_exp_f32 v13, v13\n"
    "v_exp_f32 v14, v14\n"
    "v_exp_f32 v15, v15\n"
    "v_cvt_pk_bf16_f32 v106, v12, v13\n"
    "v_cvt_pk_bf16_f32 v107, v14, v15\n"
    "v_mfma_f32_32x32x16_bf16 v[24:39], v[92:95], v[84:87], v[24:39]\n"
    "ds_read_b128 v[92:95], v121 offset:0\n"
    "v_exp_f32 v16, v16\n"
    "v_exp_f32 v17, v17\n"
    "v_exp_f32 v18, v18\n"
    "v_exp_f32 v19, v19\n"
    "v_cvt_pk_bf16_f32 v108, v16, v17\n"
    "v_cvt_pk_bf16_f32 v109, v18, v19\n"
    "v_mfma_f32_32x32x16_bf16 v[56:71], v[100:103], v[116:119], v[56:71]\n"
    "ds_read_b128 v[100:103], v122 offset:29696\n"
    "v_exp_f32 v20, v20\n"
    "v_exp_f32 v21, v21\n"
    "v_exp_f32 v22, v22\n"
    "v_exp_f32 v23, v23\n"
    "v_cvt_pk_bf16_f32 v110, v20, v21\n"
    "v_cvt_pk_bf16_f32 v111, v22, v23\n"
    "s_waitcnt lgkmcnt(3)\n"
    "v_mfma_f32_32x32x16_bf16 v[8:23], v[88:91], v[72:75], 0\n"
    "v_exp_f32 v24, v24\n"
    "v_exp_f32 v25, v25\n"
    "v_exp_f32 v26, v26\n"
    "v_exp_f32 v27, v27\n"
    "v_cvt_pk_bf16_f32 v112, v24, v25\n"
    "v_cvt_pk_bf16_f32 v113, v26, v27\n"
    "s_waitcnt lgkmcnt(2)\n"
    "v_mfma_f32_32x32x16_bf16 v[40:55], v[96:99], v[104:107], v[40:55]\n"
    "v_exp_f32 v28, v28\n"
    "v_exp_f32 v29, v29\n"
    "v_exp_f32 v30, v30\n"
    "v_exp_f32 v31, v31\n"
    "v_cvt_pk_bf16_f32 v114, v28, v29\n"
    "v_cvt_pk_bf16_f32 v115, v30, v31\n"
    "s_waitcnt lgkmcnt(1)\n"
    "v_mfma_f32_32x32x16_bf16 v[8:23], v[92:95], v[76:79], v[8:23]\n"
    "v_exp_f32 v32, v32\n"
    "v_exp_f32 v33, v33\n"
    "v_exp_f32 v34, v34\n"
    "v_exp_f32 v35, v35\n"
    "v_cvt_pk_bf16_f32 v116, v32, v33\n"
    "v_cvt_pk_bf16_f32 v117, v34, v35\n"
    "s_waitcnt lgkmcnt(0)\n"
    "v_mfma_f32_32x32x16_bf16 v[40:55], v[100:103], v[108:111], v[40:55]\n"
    "v_exp_f32 v36, v36\n"
    "v_exp_f32 v37, v37\n"
    "v_exp_f32 v38, v38\n"
    "v_exp_f32 v39, v39\n"
    "s_sub_u32 %[cnt], %[cnt], 1\n"
    "s_cmp_lg_u32 %[cnt], 0\n"
    "v_cvt_pk_bf16_f32 v118, v36, v37\n"
    "v_cvt_pk_bf16_f32 v119, v38, v39\n"
    "s_cbranch_scc1 1b\n"
    "s_barrier\n"
    "v_mfma_f32_32x32x16_bf16 v[24:39], v[88:91], v[80:83], 0\n"
    "ds_read_b128 v[88:91], v120 offset:7168\n"
    "v_exp_f32 v8, v8\n"
    "v_exp_f32 v9, v9\n"
    "v_exp_f32 v10, v10\n"
    "v_exp_f32 v11, v11\n"
    "v_cvt_pk_bf16_f32 v104, v8, v9\n"
    "v_cvt_pk_bf16_f32 v105, v10, v11\n"
    "v_mfma_f32_32x32x16_bf16 v[56:71], v[96:99], v[112:115], v[56:71]\n"
    "ds_read_b128 v[96:99], v122 offset:0\n"
    "v_exp_f32 v12, v12\n"
    "v_exp_f32 v13, v13\n"
    "v_exp_f32 v14, v14\n"
    "v_exp_f32 v15, v15\n"
    "v_cvt_pk_bf16_f32 v106, v12, v13\n"
    "v_cvt_pk_bf16_f32 v107, v14, v15\n"
    "v_mfma_f32_32x32x16_bf16 v[24:39], v[92:95], v[84:87], v[24:39]\n"
    "ds_read_b128 v[92:95], v121 offset:7168\n"
    "v_exp_f32 v16, v16\n"
    "v_exp_f32 v17, v17\n"
    "v_exp_f32 v18, v18\n"
    "v_exp_f32 v19, v19\n"
    "v_cvt_pk_bf16_f32 v108, v16, v17\n"
    "v_cvt_pk_bf16_f32 v109, v18, v19\n"
    "v_mfma_f32_32x32x16_bf16 v[56:71], v[100:103], v[116:119], v[56:71]\n"
    "ds_read_b128 v[100:103], v122 offset:1024\n"
    "v_exp_f32 v20, v20\n"
    "v_exp_f32 v21, v21\n"
    "v_exp_f32 v22, v22\n"
    "v_exp_f32 v23, v23\n"
    "v_cvt_pk_bf16_f32 v110, v20, v21\n"
    "v_cvt_pk_bf16_f32 v111, v22, v23\n"
    "s_waitcnt lgkmcnt(3)\n"
    "v_mfma_f32_32x32x16_bf16 v[8:23], v[88:91], v[72:75], 0\n"
    "v_exp_f32 v24, v24\n"
    "v_exp_f32 v25, v25\n"
    "v_exp_f32 v26, v26\n"
    "v_exp_f32 v27, v27\n"
    "v_cvt_pk_bf16_f32 v112, v24, v25\n"
    "v_cvt_pk_bf16_f32 v113, v26, v27\n"
    "s_waitcnt lgkmcnt(2)\n"
    "v_mfma_f32_32x32x16_bf16 v[40:55], v[96:99], v[104:107], v[40:55]\n"
    "v_exp_f32 v28, v28\n"
    "v_exp_f32 v29, v29\n"
    "v_exp_f32 v30, v30\n"
    "v_exp_f32 v31, v31\n"
    "v_cvt_pk_bf16_f32 v114, v28, v29\n"
    "v_cvt_pk_bf16_f32 v115, v30, v31\n"
    "s_waitcnt lgkmcnt(1)\n"
    "v_mfma_f32_32x32x16_bf16 v[8:23], v[92:95], v[76:79], v[8:23]\n"
    "v_exp_f32 v32, v32\n"
    "v_exp_f32 v33, v33\n"
    "v_exp_f32 v34, v34\n"
    "v_exp_f32 v35, v35\n"
    "v_cvt_pk_bf16_f32 v116, v32, v33\n"
    "v_cvt_pk_bf16_f32 v117, v34, v35\n"
    "s_waitcnt lgkmcnt(0)\n"
    "v_mfma_f32_32x32x16_bf16 v[40:55], v[100:103], v[108:111], v[40:55]\n"
    "v_exp_f32 v36, v36\n"
    "v_exp_f32 v37, v37\n"
    "v_exp_f32 v38, v38\n"
    "v_exp_f32 v39, v39\n"
    "v_cvt_pk_bf16_f32 v118, v36, v37\n"
    "v_cvt_pk_bf16_f32 v119, v38, v39\n"
    "s_barrier\n"
    "v_mfma_f32_32x32x16_bf16 v[24:39], v[88:91], v[80:83], 0\n"
    "ds_read_b128 v[88:91], v120 offset:14336\n"
    "v_exp_f32 v8, v8\n"
    "v_exp_f32 v9, v9\n"
    "v_exp_f32 v10, v10\n"
    "v_exp_f32 v11, v11\n"
    "v_cvt_pk_bf16_f32 v104, v8, v9\n"
    "v_cvt_pk_bf16_f32 v105, v10, v11\n"
    "v_mfma_f32_32x32x16_bf16 v[56:71], v[96:99], v[112:115], v[56:71]\n"
    "ds_read_b128 v[96:99], v122 offset:7168\n"
    "v_exp_f32 v12, v12\n"
    "v_exp_f32 v13, v13\n"
    "v_exp_f32 v14, v14\n"
    "v_exp_f32 v15, v15\n"
    "v_cvt_pk_bf16_f32 v106, v12, v13\n"
    "v_cvt_pk_bf16_f32 v107, v14, v15\n"
    "v_mfma_f32_32x32x16_bf16 v[24:39], v[92:95], v[84:87], v[24:39]\n"
    "ds_read_b128 v[92:95], v121 offset:14336\n"
    "v_exp_f32 v16, v16\n"
    "v_exp_f32 v17, v17\n"
    "v_exp_f32 v18, v18\n"
    "v_exp_f32 v19, v19\n"
    "v_cvt_pk_bf16_f32 v108, v16, v17\n"
    "v_cvt_pk_bf16_f32 v109, v18, v19\n"
    "v_mfma_f32_32x32x16_bf16 v[56:71], v[100:103], v[116:119], v[56:71]\n"
    "ds_read_b128 v[100:103], v122 offset:8192\n"
    "v_exp_f32 v20, v20\n"
    "v_exp_f32 v21, v21\n"
    "v_exp_f32 v22, v22\n"
    "v_exp_f32 v23, v23\n"
    "v_cvt_pk_bf16_f32 v110, v20, v21\n"
    "v_cvt_pk_bf16_f32 v111, v22, v23\n"
    "s_waitcnt lgkmcnt(3)\n"
    "v_mfma_f32_32x32x16_bf16 v[8:23], v[88:91], v[72:75], 0\n"
    "v_exp_f32 v24, v24\n"
    "v_exp_f32 v25, v25\n"
    "v_exp_f32 v26, v26\n"
    "v_exp_f32 v27, v27\n"
    "v_cvt_pk_bf16_f32 v112, v24, v25\n"
    "v_cvt_pk_bf16_f32 v113, v26, v27\n"
    "s_waitcnt lgkmcnt(2)\n"
    "v_mfma_f32_32x32x16_bf16 v[40:55], v[96:99], v[104:107], v[40:55]\n"
    "v_exp_f32 v28, v28\n"
    "v_exp_f32 v29, v29\n"
    "v_exp_f32 v30, v30\n"
    "v_exp_f32 v31, v31\n"
    "v_cvt_pk_bf16_f32 v114, v28, v29\n"
    "v_cvt_pk_bf16_f32 v115, v30, v31\n"
    "s_waitcnt lgkmcnt(1)\n"
    "v_mfma_f32_32x32x16_bf16 v[8:23], v[92:95], v[76:79], v[8:23]\n"
    "v_exp_f32 v32, v32\n"
    "v_exp_f32 v33, v33\n"
    "v_exp_f32 v34, v34\n"
    "v_exp_f32 v35, v35\n"
    "v_cvt_pk_bf16_f32 v116, v32, v33\n"
    "v_cvt_pk_bf16_f32 v117, v34, v35\n"
    "s_waitcnt lgkmcnt(0)\n"
    "v_mfma_f32_32x32x16_bf16 v[40:55], v[100:103], v[108:111], v[40:55]\n"
    "v_exp_f32 v36, v36\n"
    "v_exp_f32 v37, v37\n"
    "v_exp_f32 v38, v38\n"
    "v_exp_f32 v39, v39\n"
    "v_cvt_pk_bf16_f32 v118, v36, v37\n"
    "v_cvt_pk_bf16_f32 v119, v38, v39\n"
    "s_nop 1\n"
    "v_mfma_f32_32x32x16_bf16 v[56:71], v[96:99], v[112:115], v[56:71]\n"
    "v_mfma_f32_32x32x16_bf16 v[56:71], v[100:103], v[116:119], v[56:71]\n"
    "s_nop 15\n"
: "={v[40:55]}"(o0), "={v[56:71]}"(o1), [cnt] "=&s"(cnt)
: "{v[72:75]}"(q0[0]), "{v[76:79]}"(q1[0]), "{v[80:83]}"(q0[1]), "{v[84:87]}"(q1[1]), "{v120}"(ak0), "{v121}"(ak1), "{v122}"(av)
            : "memory", "scc",
"v8", "v9", "v10", "v11", "v12", "v13", "v14", "v15", "v16", "v17", "v18", "v19", "v20", "v21", "v22", "v23", "v24", "v25", "v26", "v27", "v28", "v29", "v30", "v31", "v32", "v33", "v34", "v35", "v36", "v37", "v38", "v39", "v88", "v89", "v90", "v91", "v92", "v93", "v94", "v95", "v96", "v97", "v98", "v99", "v100", "v101", "v102", "v103", "v104", "v105", "v106", "v107", "v108", "v109", "v110", "v111", "v112", "v113", "v114", "v115", "v116", "v117", "v118", "v119"
        );
    }
    const int lane2 = threadIdx.x & 63;
    volatile LAS unsigned* fl = (volatile LAS unsigned*)((lds_ptr)lds + FLAG_BASE);
    {
        lds_ptr xb = (lds_ptr)lds + (wid < 2 ? wid * 3328 : 2 * KV_STG + (wid - 2) * 3328);
        const f32x16 give = kh ? o0 : o1;
#pragma unroll
        for (int i = 0; i < 3; ++i) *(LAS f32x4*)(xb + i * 1024 + lane2 * 16) = (f32x4){give[4 * i], give[4 * i + 1], give[4 * i + 2], give[4 * i + 3]};
        *(LAS float*)(xb + 3072 + lane2 * 4) = give[12];
        asm volatile("s_waitcnt lgkmcnt(0)" ::: "memory");
        if (lane2 == 0) fl[wid] = 1u;
        while (fl[wid ^ 4] == 0u) __builtin_amdgcn_s_sleep(1);
        asm volatile("" ::: "memory");
    }
    f32x16 fin = kh ? o1 : o0;
    {
        const int pw = wid ^ 4;
        lds_cptr xb = (lds_cptr)lds + (pw < 2 ? pw * 3328 : 2 * KV_STG + (pw - 2) * 3328);
#pragma unroll
        for (int i = 0; i < 3; ++i) { const f32x4 v = *(const LAS f32x4*)(xb + i * 1024 + lane2 * 16); fin[4 * i] += v[0]; fin[4 * i + 1] += v[1]; fin[4 * i + 2] += v[2]; fin[4 * i + 3] += v[3]; }
        fin[12] += *(const LAS float*)(xb + 3072 + lane2 * 4);
    }
    const int hi2 = lane2 >> 5;
    float l = hi2 ? fin[8] : fin[12];
    bool unsafe = !(l >= 8.0e-25f && l <= 1.0e36f);
#pragma unroll
    for (int i = 0; i < 12; ++i) unsafe |= !(__builtin_fabsf(fin[i]) <= 3.0e38f);
    if (__any(unsafe)) {
        attn_exact(ws + WS_KV, kh ? q0[1] : q0[0], kh ? q1[1] : q1[0], fin, lane2);
        l = hi2 ? fin[8] : fin[12];
    }
    const float sc = gate * __builtin_amdgcn_rcpf(l);
    const __amdgpu_buffer_rsrc_t ors = mk_rsrc(ws + WS_O, 128 * 20 * 1024);
    {
        const int pw = wid ^ 4;
        lds_ptr st = (lds_ptr)lds + (pw < 2 ? pw * 3328 : 2 * KV_STG + (pw - 2) * 3328) + (lane2 & 31) * 40 + hi2 * 8;
#pragma unroll
        for (int g = 0; g < 3; ++g)
            if (g < 2 || !hi2) *(LAS u32x2*)(st + g * 16) = (u32x2){cvtpk(fin[4 * g] * sc, fin[4 * g + 1] * sc), cvtpk(fin[4 * g + 2] * sc, fin[4 * g + 3] * sc)};
    }
    asm volatile("s_waitcnt lgkmcnt(0)" ::: "memory");
    unsigned arr = 0;
    if (lane2 == 0) arr = __hip_atomic_fetch_add((LAS unsigned*)((lds_ptr)lds + FLAG_BASE + 32), 1u, __ATOMIC_RELAXED, __HIP_MEMORY_SCOPE_WORKGROUP);
    if (rfl(arr) == 7u) {
        const int t = lane2 & 7, pw = ((t >> 1) + 4 * (t & 1)) ^ 4;
        lds_cptr rb = (lds_cptr)lds + (pw < 2 ? pw * 3328 : 2 * KV_STG + (pw - 2) * 3328) + (lane2 >> 3) * 16;
        const unsigned ob = (b >> 1) * 20480 + ((b & 1) * 8 + t) * 16 + (lane2 >> 5) * 1024 + ((lane2 >> 3) & 3) * 256;
        u32x4 v[10];
#pragma unroll
        for (int n = 0; n < 10; ++n) v[n] = *(const LAS u32x4*)(rb + n * 128);
#pragma unroll
        for (int n = 0; n < 10; ++n) __builtin_amdgcn_raw_buffer_store_b128(v[n], ors, ob + n * 2048, 0, 16);
        asm volatile("s_waitcnt vmcnt(0)" ::: "memory");
        if (lane2 == 0) signal(ws, b >> 3);
    }
    const int xcd = b & 7, j = b >> 3, mt = xcd * 4 + (j >> 3), nt = j & 7;
    if (wid == 0) await(ws, mt, 8u);
    lds_bar();
    const int kg = wid & 3, th = wid >> 2;
    f32x4 acc[2][5];
    {
        bf16x8 of[2][5];
#pragma unroll
        for (int ks = 0; ks < 5; ++ks)
#pragma unroll
            for (int t = 0; t < 2; ++t)
                of[t][ks] = asfrag(__builtin_amdgcn_raw_buffer_load_b128(ors, ((mt * 4 + 2 * th + t) * 20 + kg * 5 + ks) * 1024 + lane2 * 16, 0, 16));
#pragma unroll
        for (int t = 0; t < 2; ++t)
#pragma unroll
            for (int nb = 0; nb < 5; ++nb) acc[t][nb] = (f32x4){};
        SBAR();
#pragma unroll
        for (int ks = 0; ks < 5; ++ks)
#pragma unroll
            for (int nb = 0; nb < 5; ++nb) {
                const bf16x8 wf = ldsfrag((lds_cptr)lds + W_BASE + (nb * 20 + kg * 5 + ks) * 1024 + lane2 * 16);
#pragma unroll
                for (int t = 0; t < 2; ++t) acc[t][nb] = MFMA16(wf, of[t][ks], acc[t][nb]);
            }
    }
    lds_bar();
#pragma unroll
    for (int t = 0; t < 2; ++t)
#pragma unroll
        for (int nb = 0; nb < 5; ++nb)
            *(LAS f32x4*)((lds_ptr)lds + W_BASE + (kg * 64 + (2 * th + t) * 16 + (lane2 & 15)) * P_ROW + (nb * 16 + (lane2 >> 4) * 4) * 4) = acc[t][nb];
    lds_bar();
#pragma unroll
    for (int it = 0; it < 2; ++it) {
        const int i = it * 512 + (int)threadIdx.x;
        if (i < 640) {
            const int tl = i / 10, cb = i % 10;
            lds_cptr p = (lds_cptr)lds + W_BASE + tl * P_ROW + cb * 32;
            lds_cptr yp = (lds_cptr)lds + YS_BASE + tl * P_ROW + cb * 32;
            f32x4 s0 = *(const LAS f32x4*)yp, s1 = *(const LAS f32x4*)(yp + 16);
#pragma unroll
            for (int w = 0; w < 4; ++w) { s0 += *(const LAS f32x4*)(p + w * 64 * P_ROW); s1 += *(const LAS f32x4*)(p + w * 64 * P_ROW + 16); }
            *(u32x4*)(out + (size_t)(mt * 64 + tl) * DM + nt * 80 + cb * 8) = (u32x4){cvtpk(s0[0], s0[1]), cvtpk(s0[2], s0[3]), cvtpk(s1[0], s1[1]), cvtpk(s1[2], s1[3])};
        }
    }
}

DI void gemm_role(char* lds, char* ws, int gb, int G) {
    const int lane = threadIdx.x & 63, r32 = lane & 31, hi = lane >> 5;
    const int xcd = gb & 7, j = gb >> 3, kq = xcd & 3, mt = (xcd >> 2) * 8 + (j >> 2), nt = j & 3;
    unsigned ca[4];
    const unsigned l0 = (unsigned)(uintptr_t)lds;
#pragma unroll
    for (int ss = 0; ss < 4; ++ss) ca[ss] = l0 + A_BASE + G * 4096 + r32 * 128 + (((2 * ss + hi) ^ ((r32 >> 1) & 7)) * 16);
    const unsigned ba = l0 + B_BASE + lane * 16;
    f32x16 acc[5]; unsigned gsa, gcnt;
    asm volatile("s_barrier" ::: "memory");
    asm volatile(
    "v_mov_b32 v8, 0\n"
    "v_mov_b32 v9, 0\n"
    "v_mov_b32 v10, 0\n"
    "v_mov_b32 v11, 0\n"
    "v_mov_b32 v12, 0\n"
    "v_mov_b32 v13, 0\n"
    "v_mov_b32 v14, 0\n"
    "v_mov_b32 v15, 0\n"
    "v_mov_b32 v16, 0\n"
    "v_mov_b32 v17, 0\n"
    "v_mov_b32 v18, 0\n"
    "v_mov_b32 v19, 0\n"
    "v_mov_b32 v20, 0\n"
    "v_mov_b32 v21, 0\n"
    "v_mov_b32 v22, 0\n"
    "v_mov_b32 v23, 0\n"
    "v_mov_b32 v24, 0\n"
    "v_mov_b32 v25, 0\n"
    "v_mov_b32 v26, 0\n"
    "v_mov_b32 v27, 0\n"
    "v_mov_b32 v28, 0\n"
    "v_mov_b32 v29, 0\n"
    "v_mov_b32 v30, 0\n"
    "v_mov_b32 v31, 0\n"
    "v_mov_b32 v32, 0\n"
    "v_mov_b32 v33, 0\n"
    "v_mov_b32 v34, 0\n"
    "v_mov_b32 v35, 0\n"
    "v_mov_b32 v36, 0\n"
    "v_mov_b32 v37, 0\n"
    "v_mov_b32 v38, 0\n"
    "v_mov_b32 v39, 0\n"
    "v_mov_b32 v40, 0\n"
    "v_mov_b32 v41, 0\n"
    "v_mov_b32 v42, 0\n"
    "v_mov_b32 v43, 0\n"
    "v_mov_b32 v44, 0\n"
    "v_mov_b32 v45, 0\n"
    "v_mov_b32 v46, 0\n"
    "v_mov_b32 v47, 0\n"
    "v_mov_b32 v48, 0\n"
    "v_mov_b32 v49, 0\n"
    "v_mov_b32 v50, 0\n"
    "v_mov_b32 v51, 0\n"
    "v_mov_b32 v52, 0\n"
    "v_mov_b32 v53, 0\n"
    "v_mov_b32 v54, 0\n"
    "v_mov_b32 v55, 0\n"
    "v_mov_b32 v56, 0\n"
    "v_mov_b32 v57, 0\n"
    "v_mov_b32 v58, 0\n"
    "v_mov_b32 v59, 0\n"
    "v_mov_b32 v60, 0\n"
    "v_mov_b32 v61, 0\n"
    "v_mov_b32 v62, 0\n"
    "v_mov_b32 v63, 0\n"
    "v_mov_b32 v64, 0\n"
    "v_mov_b32 v65, 0\n"
    "v_mov_b32 v66, 0\n"
    "v_mov_b32 v67, 0\n"
    "v_mov_b32 v68, 0\n"
    "v_mov_b32 v69, 0\n"
    "v_mov_b32 v70, 0\n"
    "v_mov_b32 v71, 0\n"
    "v_mov_b32 v72, 0\n"
    "v_mov_b32 v73, 0\n"
    "v_mov_b32 v74, 0\n"
    "v_mov_b32 v75, 0\n"
    "v_mov_b32 v76, 0\n"
    "v_mov_b32 v77, 0\n"
    "v_mov_b32 v78, 0\n"
    "v_mov_b32 v79, 0\n"
    "v_mov_b32 v80, 0\n"
    "v_mov_b32 v81, 0\n"
    "v_mov_b32 v82, 0\n"
    "v_mov_b32 v83, 0\n"
    "v_mov_b32 v84, 0\n"
    "v_mov_b32 v85, 0\n"
    "v_mov_b32 v86, 0\n"
    "v_mov_b32 v87, 0\n"
    "s_mov_b32 %[sa], 0\n"
    "s_mov_b32 %[cnt], 5\n"
    "s_barrier\n"
    "ds_read_b128 v[108:111], v116 offset:0\n"
    "ds_read_b128 v[88:91], v120 offset:0\n"
    "ds_read_b128 v[92:95], v120 offset:12288\n"
    "ds_read_b128 v[96:99], v120 offset:24576\n"
    "ds_read_b128 v[100:103], v120 offset:36864\n"
    "ds_read_b128 v[104:107], v120 offset:49152\n"
    "1:\n"
    "s_barrier\n"
    "v_add_u32 v121, %[sa], v117\n"
    "ds_read_b128 v[112:115], v121 offset:0\n"
    "s_waitcnt lgkmcnt(5)\n"
    "v_mfma_f32_32x32x16_bf16 v[8:23], v[88:91], v[108:111], v[8:23]\n"
    "ds_read_b128 v[88:91], v120 offset:1024\n"
    "s_waitcnt lgkmcnt(5)\n"
    "v_mfma_f32_32x32x16_bf16 v[24:39], v[92:95], v[108:111], v[24:39]\n"
    "ds_read_b128 v[92:95], v120 offset:13312\n"
    "s_waitcnt lgkmcnt(5)\n"
    "v_mfma_f32_32x32x16_bf16 v[40:55], v[96:99], v[108:111], v[40:55]\n"
    "ds_read_b128 v[96:99], v120 offset:25600\n"
    "s_waitcnt lgkmcnt(5)\n"
    "v_mfma_f32_32x32x16_bf16 v[56:71], v[100:103], v[108:111], v[56:71]\n"
    "ds_read_b128 v[100:103], v120 offset:37888\n"
    "s_waitcnt lgkmcnt(5)\n"
    "v_mfma_f32_32x32x16_bf16 v[72:87], v[104:107], v[108:111], v[72:87]\n"
    "ds_read_b128 v[104:107], v120 offset:50176\n"
    "v_add_u32 v121, %[sa], v118\n"
    "ds_read_b128 v[108:111], v121 offset:0\n"
    "s_waitcnt lgkmcnt(5)\n"
    "v_mfma_f32_32x32x16_bf16 v[8:23], v[88:91], v[112:115], v[8:23]\n"
    "ds_read_b128 v[88:91], v120 offset:2048\n"
    "s_waitcnt lgkmcnt(5)\n"
    "v_mfma_f32_32x32x16_bf16 v[24:39], v[92:95], v[112:115], v[24:39]\n"
    "ds_read_b128 v[92:95], v120 offset:14336\n"
    "s_waitcnt lgkmcnt(5)\n"
    "v_mfma_f32_32x32x16_bf16 v[40:55], v[96:99], v[112:115], v[40:55]\n"
    "ds_read_b128 v[96:99], v120 offset:26624\n"
    "s_waitcnt lgkmcnt(5)\n"
    "v_mfma_f32_32x32x16_bf16 v[56:71], v[100:103], v[112:115], v[56:71]\n"
    "ds_read_b128 v[100:103], v120 offset:38912\n"
    "s_waitcnt lgkmcnt(5)\n"
    "v_mfma_f32_32x32x16_bf16 v[72:87], v[104:107], v[112:115], v[72:87]\n"
    "ds_read_b128 v[104:107], v120 offset:51200\n"
    "v_add_u32 v121, %[sa], v119\n"
    "ds_read_b128 v[112:115], v121 offset:0\n"
    "s_waitcnt lgkmcnt(5)\n"
    "v_mfma_f32_32x32x16_bf16 v[8:23], v[88:91], v[108:111], v[8:23]\n"
    "ds_read_b128 v[88:91], v120 offset:3072\n"
    "s_waitcnt lgkmcnt(5)\n"
    "v_mfma_f32_32x32x16_bf16 v[24:39], v[92:95], v[108:111], v[24:39]\n"
    "ds_read_b128 v[92:95], v120 offset:15360\n"
    "s_waitcnt lgkmcnt(5)\n"
    "v_mfma_f32_32x32x16_bf16 v[40:55], v[96:99], v[108:111], v[40:55]\n"
    "ds_read_b128 v[96:99], v120 offset:27648\n"
    "s_waitcnt lgkmcnt(5)\n"
    "v_mfma_f32_32x32x16_bf16 v[56:71], v[100:103], v[108:111], v[56:71]\n"
    "ds_read_b128 v[100:103], v120 offset:39936\n"
    "s_waitcnt lgkmcnt(5)\n"
    "v_mfma_f32_32x32x16_bf16 v[72:87], v[104:107], v[108:111], v[72:87]\n"
    "ds_read_b128 v[104:107], v120 offset:52224\n"
    "s_barrier\n"
    "s_add_u32 %[sa], %[sa], 16384\n"
    "s_and_b32 %[sa], %[sa], 0xffff\n"
    "v_add_u32 v121, %[sa], v116\n"
    "ds_read_b128 v[108:111], v121 offset:0\n"
    "s_waitcnt lgkmcnt(5)\n"
    "v_mfma_f32_32x32x16_bf16 v[8:23], v[88:91], v[112:115], v[8:23]\n"
    "ds_read_b128 v[88:91], v120 offset:4096\n"
    "s_waitcnt lgkmcnt(5)\n"
    "v_mfma_f32_32x32x16_bf16 v[24:39], v[92:95], v[112:115], v[24:39]\n"
    "ds_read_b128 v[92:95], v120 offset:16384\n"
    "s_waitcnt lgkmcnt(5)\n"
    "v_mfma_f32_32x32x16_bf16 v[40:55], v[96:99], v[112:115], v[40:55]\n"
    "ds_read_b128 v[96:99], v120 offset:28672\n"
    "s_waitcnt lgkmcnt(5)\n"
    "v_mfma_f32_32x32x16_bf16 v[56:71], v[100:103], v[112:115], v[56:71]\n"
    "ds_read_b128 v[100:103], v120 offset:40960\n"
    "s_waitcnt lgkmcnt(5)\n"
    "v_mfma_f32_32x32x16_bf16 v[72:87], v[104:107], v[112:115], v[72:87]\n"
    "ds_read_b128 v[104:107], v120 offset:53248\n"
    "v_add_u32 v121, %[sa], v117\n"
    "ds_read_b128 v[112:115], v121 offset:0\n"
    "s_waitcnt lgkmcnt(5)\n"
    "v_mfma_f32_32x32x16_bf16 v[8:23], v[88:91], v[108:111], v[8:23]\n"
    "ds_read_b128 v[88:91], v120 offset:5120\n"
    "s_waitcnt lgkmcnt(5)\n"
    "v_mfma_f32_32x32x16_bf16 v[24:39], v[92:95], v[108:111], v[24:39]\n"
    "ds_read_b128 v[92:95], v120 offset:17408\n"
    "s_waitcnt lgkmcnt(5)\n"
    "v_mfma_f32_32x32x16_bf16 v[40:55], v[96:99], v[108:111], v[40:55]\n"
    "ds_read_b128 v[96:99], v120 offset:29696\n"
    "s_waitcnt lgkmcnt(5)\n"
    "v_mfma_f32_32x32x16_bf16 v[56:71], v[100:103], v[108:111], v[56:71]\n"
    "ds_read_b128 v[100:103], v120 offset:41984\n"
    "s_waitcnt lgkmcnt(5)\n"
    "v_mfma_f32_32x32x16_bf16 v[72:87], v[104:107], v[108:111], v[72:87]\n"
    "ds_read_b128 v[104:107], v120 offset:54272\n"
    "v_add_u32 v121, %[sa], v118\n"
    "ds_read_b128 v[108:111], v121 offset:0\n"
    "s_waitcnt lgkmcnt(5)\n"
    "v_mfma_f32_32x32x16_bf16 v[8:23], v[88:91], v[112:115], v[8:23]\n"
    "ds_read_b128 v[88:91], v120 offset:6144\n"
    "s_waitcnt lgkmcnt(5)\n"
    "v_mfma_f32_32x32x16_bf16 v[24:39], v[92:95], v[112:115], v[24:39]\n"
    "ds_read_b128 v[92:95], v120 offset:18432\n"
    "s_waitcnt lgkmcnt(5)\n"
    "v_mfma_f32_32x32x16_bf16 v[40:55], v[96:99], v[112:115], v[40:55]\n"
    "ds_read_b128 v[96:99], v120 offset:30720\n"
    "s_waitcnt lgkmcnt(5)\n"
    "v_mfma_f32_32x32x16_bf16 v[56:71], v[100:103], v[112:115], v[56:71]\n"
    "ds_read_b128 v[100:103], v120 offset:43008\n"
    "s_waitcnt lgkmcnt(5)\n"
    "v_mfma_f32_32x32x16_bf16 v[72:87], v[104:107], v[112:115], v[72:87]\n"
    "ds_read_b128 v[104:107], v120 offset:55296\n"
    "s_barrier\n"
    "v_add_u32 v121, %[sa], v119\n"
    "ds_read_b128 v[112:115], v121 offset:0\n"
    "s_waitcnt lgkmcnt(5)\n"
    "v_mfma_f32_32x32x16_bf16 v[8:23], v[88:91], v[108:111], v[8:23]\n"
    "ds_read_b128 v[88:91], v120 offset:7168\n"
    "s_waitcnt lgkmcnt(5)\n"
    "v_mfma_f32_32x32x16_bf16 v[24:39], v[92:95], v[108:111], v[24:39]\n"
    "ds_read_b128 v[92:95], v120 offset:19456\n"
    "s_waitcnt lgkmcnt(5)\n"
    "v_mfma_f32_32x32x16_bf16 v[40:55], v[96:99], v[108:111], v[40:55]\n"
    "ds_read_b128 v[96:99], v120 offset:31744\n"
    "s_waitcnt lgkmcnt(5)\n"
    "v_mfma_f32_32x32x16_bf16 v[56:71], v[100:103], v[108:111], v[56:71]\n"
    "ds_read_b128 v[100:103], v120 offset:44032\n"
    "s_waitcnt lgkmcnt(5)\n"
    "v_mfma_f32_32x32x16_bf16 v[72:87], v[104:107], v[108:111], v[72:87]\n"
    "ds_read_b128 v[104:107], v120 offset:56320\n"
    "s_add_u32 %[sa], %[sa], 16384\n"
    "s_and_b32 %[sa], %[sa], 0xffff\n"
    "v_add_u32 v121, %[sa], v116\n"
    "ds_read_b128 v[108:111], v121 offset:0\n"
    "s_waitcnt lgkmcnt(5)\n"
    "v_mfma_f32_32x32x16_bf16 v[8:23], v[88:91], v[112:115], v[8:23]\n"
    "ds_read_b128 v[88:91], v120 offset:8192\n"
    "s_waitcnt lgkmcnt(5)\n"
    "v_mfma_f32_32x32x16_bf16 v[24:39], v[92:95], v[112:115], v[24:39]\n"
    "ds_read_b128 v[92:95], v120 offset:20480\n"
    "s_waitcnt lgkmcnt(5)\n"
    "v_mfma_f32_32x32x16_bf16 v[40:55], v[96:99], v[112:115], v[40:55]\n"
    "ds_read_b128 v[96:99], v120 offset:32768\n"
    "s_waitcnt lgkmcnt(5)\n"
    "v_mfma_f32_32x32x16_bf16 v[56:71], v[100:103], v[112:115], v[56:71]\n"
    "ds_read_b128 v[100:103], v120 offset:45056\n"
    "s_waitcnt lgkmcnt(5)\n"
    "v_mfma_f32_32x32x16_bf16 v[72:87], v[104:107], v[112:115], v[72:87]\n"
    "ds_read_b128 v[104:107], v120 offset:57344\n"
    "v_add_u32 v121, %[sa], v117\n"
    "ds_read_b128 v[112:115], v121 offset:0\n"
    "s_waitcnt lgkmcnt(5)\n"
    "v_mfma_f32_32x32x16_bf16 v[8:23], v[88:91], v[108:111], v[8:23]\n"
    "ds_read_b128 v[88:91], v120 offset:9216\n"
    "s_waitcnt lgkmcnt(5)\n"
    "v_mfma_f32_32x32x16_bf16 v[24:39], v[92:95], v[108:111], v[24:39]\n"
    "ds_read_b128 v[92:95], v120 offset:21504\n"
    "s_waitcnt lgkmcnt(5)\n"
    "v_mfma_f32_32x32x16_bf16 v[40:55], v[96:99], v[108:111], v[40:55]\n"
    "ds_read_b128 v[96:99], v120 offset:33792\n"
    "s_waitcnt lgkmcnt(5)\n"
    "v_mfma_f32_32x32x16_bf16 v[56:71], v[100:103], v[108:111], v[56:71]\n"
    "ds_read_b128 v[100:103], v120 offset:46080\n"
    "s_waitcnt lgkmcnt(5)\n"
    "v_mfma_f32_32x32x16_bf16 v[72:87], v[104:107], v[108:111], v[72:87]\n"
    "ds_read_b128 v[104:107], v120 offset:58368\n"
    "s_barrier\n"
    "v_add_u32 v121, %[sa], v118\n"
    "ds_read_b128 v[108:111], v121 offset:0\n"
    "s_waitcnt lgkmcnt(5)\n"
    "v_mfma_f32_32x32x16_bf16 v[8:23], v[88:91], v[112:115], v[8:23]\n"
    "ds_read_b128 v[88:91], v120 offset:10240\n"
    "s_waitcnt lgkmcnt(5)\n"
    "v_mfma_f32_32x32x16_bf16 v[24:39], v[92:95], v[112:115], v[24:39]\n"
    "ds_read_b128 v[92:95], v120 offset:22528\n"
    "s_waitcnt lgkmcnt(5)\n"
    "v_mfma_f32_32x32x16_bf16 v[40:55], v[96:99], v[112:115], v[40:55]\n"
    "ds_read_b128 v[96:99], v120 offset:34816\n"
    "s_waitcnt lgkmcnt(5)\n"
    "v_mfma_f32_32x32x16_bf16 v[56:71], v[100:103], v[112:115], v[56:71]\n"
    "ds_read_b128 v[100:103], v120 offset:47104\n"
    "s_waitcnt lgkmcnt(5)\n"
    "v_mfma_f32_32x32x16_bf16 v[72:87], v[104:107], v[112:115], v[72:87]\n"
    "ds_read_b128 v[104:107], v120 offset:59392\n"
    "v_add_u32 v121, %[sa], v119\n"
    "ds_read_b128 v[112:115], v121 offset:0\n"
    "s_waitcnt lgkmcnt(5)\n"
    "v_mfma_f32_32x32x16_bf16 v[8:23], v[88:91], v[108:111], v[8:23]\n"
    "ds_read_b128 v[88:91], v120 offset:11264\n"
    "s_waitcnt lgkmcnt(5)\n"
    "v_mfma_f32_32x32x16_bf16 v[24:39], v[92:95], v[108:111], v[24:39]\n"
    "ds_read_b128 v[92:95], v120 offset:23552\n"
    "s_waitcnt lgkmcnt(5)\n"
    "v_mfma_f32_32x32x16_bf16 v[40:55], v[96:99], v[108:111], v[40:55]\n"
    "ds_read_b128 v[96:99], v120 offset:35840\n"
    "s_waitcnt lgkmcnt(5)\n"
    "v_mfma_f32_32x32x16_bf16 v[56:71], v[100:103], v[108:111], v[56:71]\n"
    "ds_read_b128 v[100:103], v120 offset:48128\n"
    "s_waitcnt lgkmcnt(5)\n"
    "v_mfma_f32_32x32x16_bf16 v[72:87], v[104:107], v[108:111], v[72:87]\n"
    "ds_read_b128 v[104:107], v120 offset:60416\n"
    "s_add_u32 %[sa], %[sa], 16384\n"
    "s_and_b32 %[sa], %[sa], 0xffff\n"
    "v_add_u32 v121, %[sa], v116\n"
    "ds_read_b128 v[108:111], v121 offset:0\n"
    "s_waitcnt lgkmcnt(5)\n"
    "v_mfma_f32_32x32x16_bf16 v[8:23], v[88:91], v[112:115], v[8:23]\n"
    "ds_read_b128 v[88:91], v120 offset:0\n"
    "s_waitcnt lgkmcnt(5)\n"
    "v_mfma_f32_32x32x16_bf16 v[24:39], v[92:95], v[112:115], v[24:39]\n"
    "ds_read_b128 v[92:95], v120 offset:12288\n"
    "s_waitcnt lgkmcnt(5)\n"
    "v_mfma_f32_32x32x16_bf16 v[40:55], v[96:99], v[112:115], v[40:55]\n"
    "ds_read_b128 v[96:99], v120 offset:24576\n"
    "s_waitcnt lgkmcnt(5)\n"
    "v_mfma_f32_32x32x16_bf16 v[56:71], v[100:103], v[112:115], v[56:71]\n"
    "ds_read_b128 v[100:103], v120 offset:36864\n"
    "s_waitcnt lgkmcnt(5)\n"
    "v_mfma_f32_32x32x16_bf16 v[72:87], v[104:107], v[112:115], v[72:87]\n"
    "ds_read_b128 v[104:107], v120 offset:49152\n"
    "s_sub_u32 %[cnt], %[cnt], 1\n"
    "s_cmp_lg_u32 %[cnt], 0\n"
    "s_cbranch_scc1 1b\n"
    "s_barrier\n"
    "v_add_u32 v121, %[sa], v117\n"
    "ds_read_b128 v[112:115], v121 offset:0\n"
    "s_waitcnt lgkmcnt(5)\n"
    "v_mfma_f32_32x32x16_bf16 v[8:23], v[88:91], v[108:111], v[8:23]\n"
    "ds_read_b128 v[88:91], v120 offset:1024\n"
    "s_waitcnt lgkmcnt(5)\n"
    "v_mfma_f32_32x32x16_bf16 v[24:39], v[92:95], v[108:111], v[24:39]\n"
    "ds_read_b128 v[92:95], v120 offset:13312\n"
    "s_waitcnt lgkmcnt(5)\n"
    "v_mfma_f32_32x32x16_bf16 v[40:55], v[96:99], v[108:111], v[40:55]\n"
    "ds_read_b128 v[96:99], v120 offset:25600\n"
    "s_waitcnt lgkmcnt(5)\n"
    "v_mfma_f32_32x32x16_bf16 v[56:71], v[100:103], v[108:111], v[56:71]\n"
    "ds_read_b128 v[100:103], v120 offset:37888\n"
    "s_waitcnt lgkmcnt(5)\n"
    "v_mfma_f32_32x32x16_bf16 v[72:87], v[104:107], v[108:111], v[72:87]\n"
    "ds_read_b128 v[104:107], v120 offset:50176\n"
    "v_add_u32 v121, %[sa], v118\n"
    "ds_read_b128 v[108:111], v121 offset:0\n"
    "s_waitcnt lgkmcnt(5)\n"
    "v_mfma_f32_32x32x16_bf16 v[8:23], v[88:91], v[112:115], v[8:23]\n"
    "ds_read_b128 v[88:91], v120 offset:2048\n"
    "s_waitcnt lgkmcnt(5)\n"
    "v_mfma_f32_32x32x16_bf16 v[24:39], v[92:95], v[112:115], v[24:39]\n"
    "ds_read_b128 v[92:95], v120 offset:14336\n"
    "s_waitcnt lgkmcnt(5)\n"
    "v_mfma_f32_32x32x16_bf16 v[40:55], v[96:99], v[112:115], v[40:55]\n"
    "ds_read_b128 v[96:99], v120 offset:26624\n"
    "s_waitcnt lgkmcnt(5)\n"
    "v_mfma_f32_32x32x16_bf16 v[56:71], v[100:103], v[112:115], v[56:71]\n"
    "ds_read_b128 v[100:103], v120 offset:38912\n"
    "s_waitcnt lgkmcnt(5)\n"
    "v_mfma_f32_32x32x16_bf16 v[72:87], v[104:107], v[112:115], v[72:87]\n"
    "ds_read_b128 v[104:107], v120 offset:51200\n"
    "v_add_u32 v121, %[sa], v119\n"
    "ds_read_b128 v[112:115], v121 offset:0\n"
    "s_waitcnt lgkmcnt(5)\n"
    "v_mfma_f32_32x32x16_bf16 v[8:23], v[88:91], v[108:111], v[8:23]\n"
    "ds_read_b128 v[88:91], v120 offset:3072\n"
    "s_waitcnt lgkmcnt(5)\n"
    "v_mfma_f32_32x32x16_bf16 v[24:39], v[92:95], v[108:111], v[24:39]\n"
    "ds_read_b128 v[92:95], v120 offset:15360\n"
    "s_waitcnt lgkmcnt(5)\n"
    "v_mfma_f32_32x32x16_bf16 v[40:55], v[96:99], v[108:111], v[40:55]\n"
    "ds_read_b128 v[96:99], v120 offset:27648\n"
    "s_waitcnt lgkmcnt(5)\n"
    "v_mfma_f32_32x32x16_bf16 v[56:71], v[100:103], v[108:111], v[56:71]\n"
    "ds_read_b128 v[100:103], v120 offset:39936\n"
    "s_waitcnt lgkmcnt(5)\n"
    "v_mfma_f32_32x32x16_bf16 v[72:87], v[104:107], v[108:111], v[72:87]\n"
    "ds_read_b128 v[104:107], v120 offset:52224\n"
    "s_barrier\n"
    "s_waitcnt lgkmcnt(4)\n"
    "v_mfma_f32_32x32x16_bf16 v[8:23], v[88:91], v[112:115], v[8:23]\n"
    "s_waitcnt lgkmcnt(3)\n"
    "v_mfma_f32_32x32x16_bf16 v[24:39], v[92:95], v[112:115], v[24:39]\n"
    "s_waitcnt lgkmcnt(2)\n"
    "v_mfma_f32_32x32x16_bf16 v[40:55], v[96:99], v[112:115], v[40:55]\n"
    "s_waitcnt lgkmcnt(1)\n"
    "v_mfma_f32_32x32x16_bf16 v[56:71], v[100:103], v[112:115], v[56:71]\n"
    "s_waitcnt lgkmcnt(0)\n"
    "v_mfma_f32_32x32x16_bf16 v[72:87], v[104:107], v[112:115], v[72:87]\n"
    "s_nop 15\n"
: "={v[8:23]}"(acc[0]), "={v[24:39]}"(acc[1]), "={v[40:55]}"(acc[2]), "={v[56:71]}"(acc[3]), "={v[72:87]}"(acc[4]), [sa] "=&s"(gsa), [cnt] "=&s"(gcnt)
: "{v116}"(ca[0]), "{v117}"(ca[1]), "{v118}"(ca[2]), "{v119}"(ca[3]), "{v120}"(ba)
: "memory", "scc", "v121", "v88", "v89", "v90", "v91", "v92", "v93", "v94", "v95", "v96", "v97", "v98", "v99", "v100", "v101", "v102", "v103", "v104", "v105", "v106", "v107", "v108", "v109", "v110", "v111", "v112", "v113", "v114", "v115"
    );
    const __amdgpu_buffer_rsrc_t yrs = mk_rsrc(ws + WS_Y, 4 * SEQ * ROWB);
    {
        const unsigned dst = ((kq * 64 + mt * 4 + G) * 40 + nt * 10) * 1024 + lane * 16;
#pragma unroll
        for (int nb = 0; nb < 5; ++nb) {
            const f32x16& a = acc[nb];
            asm volatile("s_barrier" ::: "memory");
#pragma unroll
            for (int gp = 0; gp < 2; ++gp) {
                unsigned x0 = cvtpk(a[8 * gp + 0], a[8 * gp + 1]), x1 = cvtpk(a[8 * gp + 2], a[8 * gp + 3]);
                unsigned y0 = cvtpk(a[8 * gp + 4], a[8 * gp + 5]), y1 = cvtpk(a[8 * gp + 6], a[8 * gp + 7]);
                swap32(x0, y0); swap32(x1, y1);
                __builtin_amdgcn_raw_buffer_store_b128((u32x4){x0, x1, y0, y1}, yrs, dst + (nb * 2 + gp) * 1024, 0, 16);
            }
        }
    }
    asm volatile("s_barrier\n\ts_barrier\n\ts_waitcnt vmcnt(0)" ::: "memory");
    if (lane == 0) {
        if (__hip_atomic_fetch_add((LAS unsigned*)((lds_ptr)lds + FLAG_BASE + 36), 1u, __ATOMIC_RELAXED, __HIP_MEMORY_SCOPE_WORKGROUP) == 3u) signal(ws, 32 + mt * 4 + nt);
    }
    const int omt = xcd * 4 + (j >> 3), ont = j & 7;
    gu32* ycnt = (gu32*)(uni(ws) + WS_CNT + (32 + (omt >> 1) * 4 + (ont >> 1)) * 128);
    asm volatile("s_barrier\n\ts_barrier" ::: "memory");
    const unsigned seen = __hip_atomic_load(ycnt, __ATOMIC_RELAXED, __HIP_MEMORY_SCOPE_AGENT);
    asm volatile("s_barrier" ::: "memory");
    if (rfl(seen) < 4u) await(ws, 32 + (omt >> 1) * 4 + (ont >> 1), 4u);
    u32x4 yp[3][4]; float gc[3];
#pragma unroll
    for (int it = 0; it < 3; ++it) {
        const int i = min(it * 256 + G * 64 + lane, 639), t = i & 31, q = i >> 5, tb = q / 10, cb = q % 10;
        const int n8 = ont * 10 + cb;
        const unsigned yo = ((omt * 2 + tb) * 40 + (n8 >> 1)) * 1024 + (n8 & 1) * 512 + t * 16;
#pragma unroll
        for (int p = 0; p < 4; ++p) yp[it][p] = __builtin_amdgcn_raw_buffer_load_b128(yrs, yo + p * 64 * 40 * 1024, 0, 16);
        gc[it] = 1.f - ((const float*)(ws + WS_GATE))[omt * 64 + tb * 32 + t];
    }
#pragma unroll
    for (int it = 0; it < 3; ++it) {
        const int i = it * 256 + G * 64 + lane;
        if (i < 640) {
            const int t = i & 31, q = i >> 5, tb = q / 10, cb = q % 10, tl = tb * 32 + t;
            float y[8];
#pragma unroll
            for (int e = 0; e < 4; ++e) {
                y[2 * e] = gc[it] * ((bflo(yp[it][0][e]) + bflo(yp[it][1][e])) + (bflo(yp[it][2][e]) + bflo(yp[it][3][e])));
                y[2 * e + 1] = gc[it] * ((bfhi(yp[it][0][e]) + bfhi(yp[it][1][e])) + (bfhi(yp[it][2][e]) + bfhi(yp[it][3][e])));
            }
            lds_ptr yd = (lds_ptr)lds + YS_BASE + tl * P_ROW + cb * 32;
            *(LAS f32x4*)yd = (f32x4){y[0], y[1], y[2], y[3]}; *(LAS f32x4*)(yd + 16) = (f32x4){y[4], y[5], y[6], y[7]};
        }
    }
    lds_bar();
}

DI void load_role(char* lds, char* ws, const u16* cre, const u16* cim, int gb, int G) {
    const int lane = threadIdx.x & 63;
    const unsigned lds0 = (unsigned)(uintptr_t)lds;
    const int xcd = gb & 7, j = gb >> 3, kq = xcd & 3, mt = (xcd >> 2) * 8 + (j >> 2), nt = j & 3;
    const char* abase = (const char*)((kq >> 1 ? cim : cre) + (size_t)mt * 128 * SEQ + (kq & 1) * 1024);
    const char* bown = ws + WS_AB + ((size_t)(nt * 5 + G) * 256 + kq * 64) * 1024;
    const int bi = min(G, SPS - 1);
    const char* bext = ws + WS_AB + ((size_t)(nt * 5 + 4) * 256 + kq * 64) * 1024;
    const int kv2 = min(4 + G, 6);
    const char* kvb = ws + WS_KV;
    unsigned voffA[4];
#pragma unroll
    for (int i = 0; i < 4; ++i) { const int row = 8 * (4 * G + i) + (lane >> 3), c = (lane & 7) ^ ((row >> 1) & 7); voffA[i] = row * 4096 + c * 16; }
    const unsigned vl = lane * 16;
    auto issue_q = [&]() {
        const char* qsrc = ws + WS_Q + (size_t)gb * 8 * ROWB;
#pragma unroll
        for (int i = 0; i < 3; ++i) { const int p = min(3 * G + i, 9); glds<1>(qsrc + p * 1024, vl, lds0 + Q_BASE + p * 1024); }
    };
    auto issue_b = [&](int q0, int slot) {
#pragma unroll
        for (int i = 0; i < SPS; ++i) glds<1>(bown + ((q0 + i) & 63) * 1024, vl, lds0 + B_BASE + (G * NBS + slot + i) * 1024);
        glds<1>(bext + ((q0 + bi) & 63) * 1024, vl, lds0 + B_BASE + (4 * NBS + slot + bi) * 1024);
    };
    auto issue_a = [&](int a) {
#pragma unroll
        for (int i = 0; i < 4; ++i) glds<1>(abase + a * 128, voffA[i], lds0 + A_BASE + (a & 3) * A_STG + (4 * G + i) * 1024);
    };
    auto issue_kv = [&](int s, int slot) {
        const char* src = kvb + s * KV_STG;
        glds<1>(src + G * 1024, vl, lds0 + slot * KV_STG + G * 1024);
        glds<1>(src + kv2 * 1024, vl, lds0 + slot * KV_STG + kv2 * 1024);
    };
    const char* wsrc = ws + WS_WO + (size_t)((j & 7) * 100 + G * 25) * 1024;
    auto issue_w4 = [&](int i) { glds<4>(wsrc + i * 4096, vl, lds0 + W_BASE + (G * 25 + i * 4) * 1024); };
    auto issue_w1 = [&](int i) { glds<1>(wsrc + i * 4096, vl, lds0 + W_BASE + (G * 25 + i * 4) * 1024); };
    issue_q();
    issue_kv(0, 0);
    issue_b(0, 0);
    issue_a(0);
    wait_vm_bar<10>();
    issue_kv(1, 1);
    issue_b(3, 3);
    issue_a(1);
    wait_vm_bar<10>();
    issue_kv(2, 2);
    issue_b(6, 6);
    issue_b(9, 9);
    issue_kv(3, 3);
    issue_a(2);
    issue_a(3);
    wait_vm_bar<24>();
    issue_kv(4, 4);
    wait_vm_bar<16>();
    issue_kv(5, 0);
    issue_b(12, 0);
    wait_vm_bar<12>();
    issue_kv(6, 1);
    issue_b(15, 3);
    issue_a(4);
    wait_vm_bar<10>();
    issue_kv(7, 2);
    issue_b(18, 6);
    issue_a(5);
    wait_vm_bar<14>();
    issue_kv(8, 3);
    issue_b(21, 9);
    issue_a(6);
    wait_vm_bar<14>();
    issue_kv(9, 4);
    issue_b(24, 0);
    wait_vm_bar<10>();
    issue_kv(10, 0);
    issue_b(27, 3);
    issue_a(7);
    wait_vm_bar<10>();
    issue_kv(11, 1);
    issue_b(30, 6);
    issue_a(8);
    wait_vm_bar<14>();
    issue_kv(12, 2);
    issue_b(33, 9);
    issue_a(9);
    wait_vm_bar<14>();
    issue_kv(13, 3);
    issue_b(36, 0);
    wait_vm_bar<10>();
    issue_kv(14, 4);
    issue_b(39, 3);
    issue_a(10);
    wait_vm_bar<10>();
    issue_kv(15, 0);
    issue_b(42, 6);
    issue_a(11);
    wait_vm_bar<14>();
    issue_kv(16, 1);
    issue_b(45, 9);
    issue_a(12);
    wait_vm_bar<14>();
    issue_kv(17, 2);
    issue_b(48, 0);
    wait_vm_bar<10>();
    issue_kv(18, 3);
    issue_b(51, 3);
    issue_a(13);
    wait_vm_bar<10>();
    issue_kv(19, 4);
    issue_b(54, 6);
    issue_a(14);
    wait_vm_bar<14>();
    issue_kv(20, 0);
    issue_b(57, 9);
    issue_a(15);
    wait_vm_bar<14>();
    issue_kv(21, 1);
    issue_b(60, 0);
    wait_vm_bar<10>();
    issue_kv(22, 2);
    issue_b(63, 3);
    wait_vm_bar<6>();
    issue_kv(23, 3);
    wait_vm_bar<2>();
    issue_kv(24, 4);
    wait_vm_bar<4>();
    issue_kv(25, 0);
    wait_vm_bar<4>();
    issue_kv(26, 1);
    issue_w4(0);
    wait_vm_bar<8>();
    issue_kv(27, 2);
    issue_w4(1);
    wait_vm_bar<12>();
    issue_kv(28, 3);
    issue_w4(2);
    wait_vm_bar<16>();
    issue_kv(29, 4);
    issue_w4(3);
    wait_vm_bar<16>();
    issue_kv(30, 0);
    issue_w4(4);
    wait_vm_bar<16>();
    issue_kv(31, 1);
    issue_w4(5);
    wait_vm_bar<16>();
    issue_w1(6);
    wait_vm_bar<11>();
    wait_vm_bar<5>();
    wait_vm_bar<0>();
    asm volatile("s_waitcnt vmcnt(0)" ::: "memory");
    asm volatile("s_barrier" ::: "memory");
}

__global__ __launch_bounds__(1024, 4) void k2_mix(char* ws, const u16* cre, const u16* cim, u16* out) {
    extern __shared__ __attribute__((aligned(16))) char lds[];
    const int wid = rfl(threadIdx.x >> 6);
    if (wid < 8) attn_role(lds, ws, out, blockIdx.x);
    else if (wid < 12) gemm_role(lds, ws, blockIdx.x, wid - 8);
    else {
        __builtin_amdgcn_s_setprio(3);
        load_role(lds, ws, cre, cim, blockIdx.x, wid - 12);
    }
}

extern "C" void kernel_launch(void* const* d_in, const int* in_sizes, int n_in, void* d_out, int out_size, void* d_ws, size_t ws_size, hipStream_t stream) {
    const u16* xA = (const u16*)d_in[0]; const u16* wq = (const u16*)d_in[1]; const u16* wk = (const u16*)d_in[2]; const u16* wv = (const u16*)d_in[3];
    const u16* wo = (const u16*)d_in[4]; const u16* cre = (const u16*)d_in[6]; const u16* cim = (const u16*)d_in[7];
    const u16* hre = (const u16*)d_in[8]; const u16* him = (const u16*)d_in[9]; const u16* gw = (const u16*)d_in[10];
    char* ws = (char*)d_ws;
    (void)hipFuncSetAttribute((const void*)k1_proj, hipFuncAttributeMaxDynamicSharedMemorySize, K1_LDS);
    (void)hipFuncSetAttribute((const void*)k2_mix, hipFuncAttributeMaxDynamicSharedMemorySize, K2_LDS);
    k1_proj<<<256, 512, K1_LDS, stream>>>(xA, wq, wk, wv, wo, hre, him, gw, ws);
    k2_mix<<<256, 1024, K2_LDS, stream>>>(ws, cre, cim, (u16*)d_out);
}
```
